```python
import torch

def _softmax(x, axis=-1):
    return torch.softmax(x, dim=axis)

def _gelu(x):
    return torch.nn.functional.gelu(x, approximate='tanh')

def _silu(x):
    return torch.nn.functional.silu(x)

def _sigmoid(x):
    return torch.sigmoid(x)

def _layernorm(x, axis=-1, eps=1e-05):
    m = x.mean(dim=axis, keepdim=True)
    v = x.var(dim=axis, keepdim=True, unbiased=False)
    return (x - m) / torch.sqrt(v + eps)

def _scatter_add(tgt, idx, src, axis=0):
    return tgt.clone().index_add_(axis, idx, src)

def _segment_sum(src, seg, n):
    out = torch.zeros((n,) + src.shape[1:], dtype=src.dtype)
    return out.index_add_(0, seg, src)

def _pad(x, pw):
    flat = tuple((v for pair in reversed(pw) for v in pair))
    return torch.nn.functional.pad(x, flat)
kernel_name = 'adaLN_cond__spectral_gfnet__dense_activation_silu__prompt'

def setup_inputs():

    _w = lambda a: (lambda t: t.to(torch.bfloat16) if t.is_floating_point() else t)(torch.as_tensor(a))

    def _A_setup():
        B, S, D = (1, 2048, 576)
        Df = D // 2 + 1
        return {'x': _w((lambda *s: torch.randn(*s, dtype=torch.bfloat16) * 0.05)(B, S, D)), 'w_r': _w((lambda *s: torch.randn(*s, dtype=torch.bfloat16) * 1.6)(S, Df) * 0.02), 'w_i': _w((lambda *s: torch.randn(*s, dtype=torch.bfloat16) * 1.6)(S, Df) * 0.02)}
    _A_in = _A_setup()

    def _B_setup():
        B, S, D, F = (1, 2048, 576, 4608)
        return {'x': _w((lambda *s: torch.randn(*s, dtype=torch.bfloat16) * 0.05)(B, S, D)), 'w1': _w((lambda *s: torch.randn(*s, dtype=torch.bfloat16) * 0.8)(D, F) * D ** (-0.5)), 'w2': _w((lambda *s: torch.randn(*s, dtype=torch.bfloat16) * 0.2)(F, D) * F ** (-0.5))}
    _B_in = _B_setup()
    return {'A_x': _A_in['x'], 'A_w_r': _A_in['w_r'], 'A_w_i': _A_in['w_i'], 'B_x': _B_in['x'], 'B_w1': _B_in['w1'], 'B_w2': _B_in['w2'], 'c': _w((lambda *s: torch.randn(*s, dtype=torch.bfloat16) * 0.2)(1, 576) * 8.0), 'w_mod': _w((lambda *s: torch.randn(*s, dtype=torch.bfloat16) * 0.2)(576, 6 * 576) * 576 ** (-0.5) * 8.0)}

def reference(A_x, A_w_r, A_w_i, B_x, B_w1, B_w2, c, w_mod):

    def _A_ref(x, w_r, w_i):
        B, S, D = x.shape
        X = (lambda x, axes=(-2, -1): torch.fft.rfft2(x.float(), dim=axes))(x, axes=(-2, -1))
        W = w_r + 1j * w_i
        X = X * W
        y = (lambda x, s, axes=(-2, -1): torch.fft.irfft2(x, s=s, dim=axes).to(torch.bfloat16))(X, (S, D), axes=(-2, -1))
        return y

    def _B_ref(x, w1, w2):
        h = torch.einsum('bsd,df->bsf', x, w1)
        h = _silu(h)
        out = torch.einsum('bsf,fd->bsd', h, w2)
        return out
    x = A_x
    mod = (_silu(c) @ w_mod).reshape(c.shape[0], 6, 1, -1)
    sa, ca, ga, sb, cb, gb = (mod[:, 0].clone(), mod[:, 1].clone(), mod[:, 2].clone(), mod[:, 3].clone(), mod[:, 4].clone(), mod[:, 5].clone())
    h = _layernorm(x, axis=-1) * (1 + ca) + sa
    x = x + ga * _A_ref(h, A_w_r, A_w_i)
    h = _layernorm(x, axis=-1) * (1 + cb) + sb
    x = x + gb * _B_ref(h, B_w1, B_w2)
    return x
```

```cpp
#include <hip/hip_runtime.h>
#include <stdint.h>

typedef unsigned short u16;
typedef short bf16x8 __attribute__((ext_vector_type(8)));
typedef short bf16x4 __attribute__((ext_vector_type(4)));
typedef float f32x4 __attribute__((ext_vector_type(4)));

constexpr int S_ = 2048, D_ = 576, DF_ = 289, F_ = 4608;

__device__ __attribute__((aligned(16))) const float TW2048[4096] = {
  1.000000000e+00f,0.000000000e+00f,9.999952912e-01f,-3.067956772e-03f,9.999811649e-01f,-6.135884672e-03f,9.999576211e-01f,-9.203754365e-03f,
  9.999247193e-01f,-1.227153838e-02f,9.998823404e-01f,-1.533920597e-02f,9.998306036e-01f,-1.840673015e-02f,9.997693896e-01f,-2.147408016e-02f,
  9.996988177e-01f,-2.454122901e-02f,9.996188283e-01f,-2.760814503e-02f,9.995294213e-01f,-3.067480400e-02f,9.994305968e-01f,-3.374117240e-02f,
  9.993223548e-01f,-3.680722415e-02f,9.992047548e-01f,-3.987292573e-02f,9.990777373e-01f,-4.293825850e-02f,9.989413023e-01f,-4.600318149e-02f,
  9.987954497e-01f,-4.906767607e-02f,9.986402392e-01f,-5.213170499e-02f,9.984755516e-01f,-5.519524589e-02f,9.983015656e-01f,-5.825826526e-02f,
  9.981181026e-01f,-6.132073700e-02f,9.979252815e-01f,-6.438262761e-02f,9.977230430e-01f,-6.744392216e-02f,9.975114465e-01f,-7.050457597e-02f,
  9.972904325e-01f,-7.356456667e-02f,9.970600605e-01f,-7.662386447e-02f,9.968202710e-01f,-7.968243957e-02f,9.965711236e-01f,-8.274026215e-02f,
  9.963126183e-01f,-8.579730988e-02f,9.960446954e-01f,-8.885355294e-02f,9.957674146e-01f,-9.190895408e-02f,9.954807758e-01f,-9.496349841e-02f,
  9.951847196e-01f,-9.801714122e-02f,9.948793054e-01f,-1.010698602e-01f,9.945645928e-01f,-1.041216329e-01f,9.942404628e-01f,-1.071724221e-01f,
  9.939069748e-01f,-1.102222055e-01f,9.935641289e-01f,-1.132709533e-01f,9.932119250e-01f,-1.163186282e-01f,9.928504229e-01f,-1.193652153e-01f,
  9.924795628e-01f,-1.224106774e-01f,9.920992851e-01f,-1.254549772e-01f,9.917097688e-01f,-1.284981072e-01f,9.913108349e-01f,-1.315400302e-01f,
  9.909026623e-01f,-1.345807016e-01f,9.904850721e-01f,-1.376201212e-01f,9.900581837e-01f,-1.406582445e-01f,9.896219969e-01f,-1.436950266e-01f,
  9.891765118e-01f,-1.467304677e-01f,9.887216687e-01f,-1.497645378e-01f,9.882575870e-01f,-1.527971923e-01f,9.877841473e-01f,-1.558284014e-01f,
  9.873014092e-01f,-1.588581502e-01f,9.868093729e-01f,-1.618863940e-01f,9.863080978e-01f,-1.649131179e-01f,9.857975245e-01f,-1.679382920e-01f,
  9.852776527e-01f,-1.709618866e-01f,9.847484827e-01f,-1.739838719e-01f,9.842100739e-01f,-1.770042181e-01f,9.836624265e-01f,-1.800228953e-01f,
  9.831054807e-01f,-1.830398887e-01f,9.825392962e-01f,-1.860551536e-01f,9.819638729e-01f,-1.890686601e-01f,9.813792109e-01f,-1.920803934e-01f,
  9.807852507e-01f,-1.950903237e-01f,9.801821113e-01f,-1.980984062e-01f,9.795697927e-01f,-2.011046410e-01f,9.789481759e-01f,-2.041089684e-01f,
  9.783173800e-01f,-2.071113735e-01f,9.776773453e-01f,-2.101118416e-01f,9.770281315e-01f,-2.131103128e-01f,9.763697386e-01f,-2.161068022e-01f,
  9.757021070e-01f,-2.191012353e-01f,9.750253558e-01f,-2.220936269e-01f,9.743393660e-01f,-2.250839174e-01f,9.736442566e-01f,-2.280720770e-01f,
  9.729399681e-01f,-2.310581058e-01f,9.722265005e-01f,-2.340419590e-01f,9.715039134e-01f,-2.370236069e-01f,9.707721472e-01f,-2.400030196e-01f,
  9.700312614e-01f,-2.429801822e-01f,9.692812562e-01f,-2.459550500e-01f,9.685220718e-01f,-2.489276081e-01f,9.677538276e-01f,-2.518978119e-01f,
  9.669764638e-01f,-2.548656464e-01f,9.661899805e-01f,-2.578310966e-01f,9.653944373e-01f,-2.607941031e-01f,9.645897746e-01f,-2.637546659e-01f,
  9.637760520e-01f,-2.667127550e-01f,9.629532695e-01f,-2.696683109e-01f,9.621214271e-01f,-2.726213634e-01f,9.612804651e-01f,-2.755718231e-01f,
  9.604305029e-01f,-2.785196900e-01f,9.595715404e-01f,-2.814649343e-01f,9.587034583e-01f,-2.844075263e-01f,9.578264356e-01f,-2.873474658e-01f,
  9.569403529e-01f,-2.902846634e-01f,9.560452700e-01f,-2.932191491e-01f,9.551411867e-01f,-2.961508930e-01f,9.542281032e-01f,-2.990798354e-01f,
  9.533060193e-01f,-3.020059466e-01f,9.523749948e-01f,-3.049292266e-01f,9.514350295e-01f,-3.078496456e-01f,9.504860640e-01f,-3.107671440e-01f,
  9.495281577e-01f,-3.136817515e-01f,9.485613704e-01f,-3.165933788e-01f,9.475855827e-01f,-3.195020258e-01f,9.466009140e-01f,-3.224076927e-01f,
  9.456073046e-01f,-3.253102899e-01f,9.446048141e-01f,-3.282098472e-01f,9.435934424e-01f,-3.311063051e-01f,9.425731897e-01f,-3.339996636e-01f,
  9.415440559e-01f,-3.368898630e-01f,9.405060410e-01f,-3.397768736e-01f,9.394592047e-01f,-3.426607251e-01f,9.384035468e-01f,-3.455413282e-01f,
  9.373390079e-01f,-3.484186828e-01f,9.362656474e-01f,-3.512927592e-01f,9.351835251e-01f,-3.541635275e-01f,9.340925217e-01f,-3.570309579e-01f,
  9.329928160e-01f,-3.598950505e-01f,9.318842888e-01f,-3.627557158e-01f,9.307669401e-01f,-3.656129837e-01f,9.296408892e-01f,-3.684668243e-01f,
  9.285060763e-01f,-3.713172078e-01f,9.273625016e-01f,-3.741640747e-01f,9.262102246e-01f,-3.770074248e-01f,9.250492454e-01f,-3.798471987e-01f,
  9.238795042e-01f,-3.826834261e-01f,9.227011204e-01f,-3.855160475e-01f,9.215140343e-01f,-3.883450329e-01f,9.203183055e-01f,-3.911703825e-01f,
  9.191138744e-01f,-3.939920366e-01f,9.179008007e-01f,-3.968099952e-01f,9.166790843e-01f,-3.996241987e-01f,9.154487252e-01f,-4.024346471e-01f,
  9.142097831e-01f,-4.052413106e-01f,9.129621983e-01f,-4.080441594e-01f,9.117060304e-01f,-4.108431637e-01f,9.104412794e-01f,-4.136383235e-01f,
  9.091680050e-01f,-4.164295495e-01f,9.078860879e-01f,-4.192169011e-01f,9.065957069e-01f,-4.220002592e-01f,9.052967429e-01f,-4.247796834e-01f,
  9.039893150e-01f,-4.275550842e-01f,9.026733041e-01f,-4.303264916e-01f,9.013488293e-01f,-4.330938160e-01f,9.000158906e-01f,-4.358570874e-01f,
  8.986744881e-01f,-4.386162460e-01f,8.973245621e-01f,-4.413712621e-01f,8.959662318e-01f,-4.441221356e-01f,8.945994973e-01f,-4.468688369e-01f,
  8.932242990e-01f,-4.496113360e-01f,8.918406963e-01f,-4.523495734e-01f,8.904487491e-01f,-4.550835788e-01f,8.890483379e-01f,-4.578132927e-01f,
  8.876396418e-01f,-4.605387151e-01f,8.862225413e-01f,-4.632597864e-01f,8.847970963e-01f,-4.659765065e-01f,8.833633661e-01f,-4.686888158e-01f,
  8.819212914e-01f,-4.713967443e-01f,8.804708719e-01f,-4.741002023e-01f,8.790122271e-01f,-4.767992198e-01f,8.775452971e-01f,-4.794937670e-01f,
  8.760700822e-01f,-4.821837842e-01f,8.745866418e-01f,-4.848692417e-01f,8.730949759e-01f,-4.875501692e-01f,8.715950847e-01f,-4.902264774e-01f,
  8.700869679e-01f,-4.928981960e-01f,8.685706854e-01f,-4.955652654e-01f,8.670462370e-01f,-4.982276559e-01f,8.655136228e-01f,-5.008853674e-01f,
  8.639728427e-01f,-5.035383701e-01f,8.624239564e-01f,-5.061866641e-01f,8.608669639e-01f,-5.088301301e-01f,8.593018055e-01f,-5.114688277e-01f,
  8.577286005e-01f,-5.141027570e-01f,8.561473489e-01f,-5.167317986e-01f,8.545579910e-01f,-5.193560123e-01f,8.529605865e-01f,-5.219752789e-01f,
  8.513551950e-01f,-5.245896578e-01f,8.497417569e-01f,-5.271991491e-01f,8.481203318e-01f,-5.298036337e-01f,8.464909196e-01f,-5.324031115e-01f,
  8.448535800e-01f,-5.349976420e-01f,8.432082534e-01f,-5.375870466e-01f,8.415549994e-01f,-5.401714444e-01f,8.398938179e-01f,-5.427507758e-01f,
  8.382247090e-01f,-5.453249812e-01f,8.365477324e-01f,-5.478940606e-01f,8.348628879e-01f,-5.504579544e-01f,8.331701756e-01f,-5.530167222e-01f,
  8.314695954e-01f,-5.555702448e-01f,8.297612071e-01f,-5.581185222e-01f,8.280450702e-01f,-5.606615543e-01f,8.263210654e-01f,-5.631993413e-01f,
  8.245893121e-01f,-5.657318234e-01f,8.228498101e-01f,-5.682589412e-01f,8.211025000e-01f,-5.707807541e-01f,8.193475008e-01f,-5.732971430e-01f,
  8.175848126e-01f,-5.758081675e-01f,8.158144355e-01f,-5.783137679e-01f,8.140363097e-01f,-5.808139443e-01f,8.122506142e-01f,-5.833086371e-01f,
  8.104571700e-01f,-5.857978463e-01f,8.086561561e-01f,-5.882815719e-01f,8.068475723e-01f,-5.907596946e-01f,8.050313592e-01f,-5.932322741e-01f,
  8.032075167e-01f,-5.956993103e-01f,8.013761640e-01f,-5.981606841e-01f,7.995372415e-01f,-6.006164551e-01f,7.976908684e-01f,-6.030666232e-01f,
  7.958369255e-01f,-6.055110693e-01f,7.939754725e-01f,-6.079497933e-01f,7.921065688e-01f,-6.103827953e-01f,7.902302146e-01f,-6.128100753e-01f,
  7.883464098e-01f,-6.152315736e-01f,7.864552140e-01f,-6.176472902e-01f,7.845565677e-01f,-6.200572252e-01f,7.826505899e-01f,-6.224612594e-01f,
  7.807372212e-01f,-6.248595119e-01f,7.788165212e-01f,-6.272518039e-01f,7.768884897e-01f,-6.296382546e-01f,7.749531269e-01f,-6.320187449e-01f,
  7.730104327e-01f,-6.343932748e-01f,7.710605264e-01f,-6.367618442e-01f,7.691033483e-01f,-6.391244531e-01f,7.671388984e-01f,-6.414810419e-01f,
  7.651672363e-01f,-6.438315511e-01f,7.631884217e-01f,-6.461760402e-01f,7.612023950e-01f,-6.485143900e-01f,7.592092156e-01f,-6.508466601e-01f,
  7.572088242e-01f,-6.531728506e-01f,7.552013993e-01f,-6.554928422e-01f,7.531868219e-01f,-6.578066945e-01f,7.511651516e-01f,-6.601143479e-01f,
  7.491363883e-01f,-6.624158025e-01f,7.471005917e-01f,-6.647109985e-01f,7.450577617e-01f,-6.669999361e-01f,7.430079579e-01f,-6.692826152e-01f,
  7.409511209e-01f,-6.715589762e-01f,7.388873100e-01f,-6.738290191e-01f,7.368165851e-01f,-6.760926843e-01f,7.347388864e-01f,-6.783500314e-01f,
  7.326542735e-01f,-6.806010008e-01f,7.305627465e-01f,-6.828455329e-01f,7.284643650e-01f,-6.850836873e-01f,7.263591290e-01f,-6.873153448e-01f,
  7.242470980e-01f,-6.895405650e-01f,7.221282125e-01f,-6.917592287e-01f,7.200025320e-01f,-6.939714551e-01f,7.178700566e-01f,-6.961771250e-01f,
  7.157308459e-01f,-6.983762383e-01f,7.135848403e-01f,-7.005687952e-01f,7.114322186e-01f,-7.027547359e-01f,7.092728019e-01f,-7.049340606e-01f,
  7.071067691e-01f,-7.071067691e-01f,7.049340606e-01f,-7.092728019e-01f,7.027547359e-01f,-7.114322186e-01f,7.005687952e-01f,-7.135848403e-01f,
  6.983762383e-01f,-7.157308459e-01f,6.961771250e-01f,-7.178700566e-01f,6.939714551e-01f,-7.200025320e-01f,6.917592287e-01f,-7.221282125e-01f,
  6.895405650e-01f,-7.242470980e-01f,6.873153448e-01f,-7.263591290e-01f,6.850836873e-01f,-7.284643650e-01f,6.828455329e-01f,-7.305627465e-01f,
  6.806010008e-01f,-7.326542735e-01f,6.783500314e-01f,-7.347388864e-01f,6.760926843e-01f,-7.368165851e-01f,6.738290191e-01f,-7.388873100e-01f,
  6.715589762e-01f,-7.409511209e-01f,6.692826152e-01f,-7.430079579e-01f,6.669999361e-01f,-7.450577617e-01f,6.647109985e-01f,-7.471005917e-01f,
  6.624158025e-01f,-7.491363883e-01f,6.601143479e-01f,-7.511651516e-01f,6.578066945e-01f,-7.531868219e-01f,6.554928422e-01f,-7.552013993e-01f,
  6.531728506e-01f,-7.572088242e-01f,6.508466601e-01f,-7.592092156e-01f,6.485143900e-01f,-7.612023950e-01f,6.461760402e-01f,-7.631884217e-01f,
  6.438315511e-01f,-7.651672363e-01f,6.414810419e-01f,-7.671388984e-01f,6.391244531e-01f,-7.691033483e-01f,6.367618442e-01f,-7.710605264e-01f,
  6.343932748e-01f,-7.730104327e-01f,6.320187449e-01f,-7.749531269e-01f,6.296382546e-01f,-7.768884897e-01f,6.272518039e-01f,-7.788165212e-01f,
  6.248595119e-01f,-7.807372212e-01f,6.224612594e-01f,-7.826505899e-01f,6.200572252e-01f,-7.845565677e-01f,6.176472902e-01f,-7.864552140e-01f,
  6.152315736e-01f,-7.883464098e-01f,6.128100753e-01f,-7.902302146e-01f,6.103827953e-01f,-7.921065688e-01f,6.079497933e-01f,-7.939754725e-01f,
  6.055110693e-01f,-7.958369255e-01f,6.030666232e-01f,-7.976908684e-01f,6.006164551e-01f,-7.995372415e-01f,5.981606841e-01f,-8.013761640e-01f,
  5.956993103e-01f,-8.032075167e-01f,5.932322741e-01f,-8.050313592e-01f,5.907596946e-01f,-8.068475723e-01f,5.882815719e-01f,-8.086561561e-01f,
  5.857978463e-01f,-8.104571700e-01f,5.833086371e-01f,-8.122506142e-01f,5.808139443e-01f,-8.140363097e-01f,5.783137679e-01f,-8.158144355e-01f,
  5.758081675e-01f,-8.175848126e-01f,5.732971430e-01f,-8.193475008e-01f,5.707807541e-01f,-8.211025000e-01f,5.682589412e-01f,-8.228498101e-01f,
  5.657318234e-01f,-8.245893121e-01f,5.631993413e-01f,-8.263210654e-01f,5.606615543e-01f,-8.280450702e-01f,5.581185222e-01f,-8.297612071e-01f,
  5.555702448e-01f,-8.314695954e-01f,5.530167222e-01f,-8.331701756e-01f,5.504579544e-01f,-8.348628879e-01f,5.478940606e-01f,-8.365477324e-01f,
  5.453249812e-01f,-8.382247090e-01f,5.427507758e-01f,-8.398938179e-01f,5.401714444e-01f,-8.415549994e-01f,5.375870466e-01f,-8.432082534e-01f,
  5.349976420e-01f,-8.448535800e-01f,5.324031115e-01f,-8.464909196e-01f,5.298036337e-01f,-8.481203318e-01f,5.271991491e-01f,-8.497417569e-01f,
  5.245896578e-01f,-8.513551950e-01f,5.219752789e-01f,-8.529605865e-01f,5.193560123e-01f,-8.545579910e-01f,5.167317986e-01f,-8.561473489e-01f,
  5.141027570e-01f,-8.577286005e-01f,5.114688277e-01f,-8.593018055e-01f,5.088301301e-01f,-8.608669639e-01f,5.061866641e-01f,-8.624239564e-01f,
  5.035383701e-01f,-8.639728427e-01f,5.008853674e-01f,-8.655136228e-01f,4.982276559e-01f,-8.670462370e-01f,4.955652654e-01f,-8.685706854e-01f,
  4.928981960e-01f,-8.700869679e-01f,4.902264774e-01f,-8.715950847e-01f,4.875501692e-01f,-8.730949759e-01f,4.848692417e-01f,-8.745866418e-01f,
  4.821837842e-01f,-8.760700822e-01f,4.794937670e-01f,-8.775452971e-01f,4.767992198e-01f,-8.790122271e-01f,4.741002023e-01f,-8.804708719e-01f,
  4.713967443e-01f,-8.819212914e-01f,4.686888158e-01f,-8.833633661e-01f,4.659765065e-01f,-8.847970963e-01f,4.632597864e-01f,-8.862225413e-01f,
  4.605387151e-01f,-8.876396418e-01f,4.578132927e-01f,-8.890483379e-01f,4.550835788e-01f,-8.904487491e-01f,4.523495734e-01f,-8.918406963e-01f,
  4.496113360e-01f,-8.932242990e-01f,4.468688369e-01f,-8.945994973e-01f,4.441221356e-01f,-8.959662318e-01f,4.413712621e-01f,-8.973245621e-01f,
  4.386162460e-01f,-8.986744881e-01f,4.358570874e-01f,-9.000158906e-01f,4.330938160e-01f,-9.013488293e-01f,4.303264916e-01f,-9.026733041e-01f,
  4.275550842e-01f,-9.039893150e-01f,4.247796834e-01f,-9.052967429e-01f,4.220002592e-01f,-9.065957069e-01f,4.192169011e-01f,-9.078860879e-01f,
  4.164295495e-01f,-9.091680050e-01f,4.136383235e-01f,-9.104412794e-01f,4.108431637e-01f,-9.117060304e-01f,4.080441594e-01f,-9.129621983e-01f,
  4.052413106e-01f,-9.142097831e-01f,4.024346471e-01f,-9.154487252e-01f,3.996241987e-01f,-9.166790843e-01f,3.968099952e-01f,-9.179008007e-01f,
  3.939920366e-01f,-9.191138744e-01f,3.911703825e-01f,-9.203183055e-01f,3.883450329e-01f,-9.215140343e-01f,3.855160475e-01f,-9.227011204e-01f,
  3.826834261e-01f,-9.238795042e-01f,3.798471987e-01f,-9.250492454e-01f,3.770074248e-01f,-9.262102246e-01f,3.741640747e-01f,-9.273625016e-01f,
  3.713172078e-01f,-9.285060763e-01f,3.684668243e-01f,-9.296408892e-01f,3.656129837e-01f,-9.307669401e-01f,3.627557158e-01f,-9.318842888e-01f,
  3.598950505e-01f,-9.329928160e-01f,3.570309579e-01f,-9.340925217e-01f,3.541635275e-01f,-9.351835251e-01f,3.512927592e-01f,-9.362656474e-01f,
  3.484186828e-01f,-9.373390079e-01f,3.455413282e-01f,-9.384035468e-01f,3.426607251e-01f,-9.394592047e-01f,3.397768736e-01f,-9.405060410e-01f,
  3.368898630e-01f,-9.415440559e-01f,3.339996636e-01f,-9.425731897e-01f,3.311063051e-01f,-9.435934424e-01f,3.282098472e-01f,-9.446048141e-01f,
  3.253102899e-01f,-9.456073046e-01f,3.224076927e-01f,-9.466009140e-01f,3.195020258e-01f,-9.475855827e-01f,3.165933788e-01f,-9.485613704e-01f,
  3.136817515e-01f,-9.495281577e-01f,3.107671440e-01f,-9.504860640e-01f,3.078496456e-01f,-9.514350295e-01f,3.049292266e-01f,-9.523749948e-01f,
  3.020059466e-01f,-9.533060193e-01f,2.990798354e-01f,-9.542281032e-01f,2.961508930e-01f,-9.551411867e-01f,2.932191491e-01f,-9.560452700e-01f,
  2.902846634e-01f,-9.569403529e-01f,2.873474658e-01f,-9.578264356e-01f,2.844075263e-01f,-9.587034583e-01f,2.814649343e-01f,-9.595715404e-01f,
  2.785196900e-01f,-9.604305029e-01f,2.755718231e-01f,-9.612804651e-01f,2.726213634e-01f,-9.621214271e-01f,2.696683109e-01f,-9.629532695e-01f,
  2.667127550e-01f,-9.637760520e-01f,2.637546659e-01f,-9.645897746e-01f,2.607941031e-01f,-9.653944373e-01f,2.578310966e-01f,-9.661899805e-01f,
  2.548656464e-01f,-9.669764638e-01f,2.518978119e-01f,-9.677538276e-01f,2.489276081e-01f,-9.685220718e-01f,2.459550500e-01f,-9.692812562e-01f,
  2.429801822e-01f,-9.700312614e-01f,2.400030196e-01f,-9.707721472e-01f,2.370236069e-01f,-9.715039134e-01f,2.340419590e-01f,-9.722265005e-01f,
  2.310581058e-01f,-9.729399681e-01f,2.280720770e-01f,-9.736442566e-01f,2.250839174e-01f,-9.743393660e-01f,2.220936269e-01f,-9.750253558e-01f,
  2.191012353e-01f,-9.757021070e-01f,2.161068022e-01f,-9.763697386e-01f,2.131103128e-01f,-9.770281315e-01f,2.101118416e-01f,-9.776773453e-01f,
  2.071113735e-01f,-9.783173800e-01f,2.041089684e-01f,-9.789481759e-01f,2.011046410e-01f,-9.795697927e-01f,1.980984062e-01f,-9.801821113e-01f,
  1.950903237e-01f,-9.807852507e-01f,1.920803934e-01f,-9.813792109e-01f,1.890686601e-01f,-9.819638729e-01f,1.860551536e-01f,-9.825392962e-01f,
  1.830398887e-01f,-9.831054807e-01f,1.800228953e-01f,-9.836624265e-01f,1.770042181e-01f,-9.842100739e-01f,1.739838719e-01f,-9.847484827e-01f,
  1.709618866e-01f,-9.852776527e-01f,1.679382920e-01f,-9.857975245e-01f,1.649131179e-01f,-9.863080978e-01f,1.618863940e-01f,-9.868093729e-01f,
  1.588581502e-01f,-9.873014092e-01f,1.558284014e-01f,-9.877841473e-01f,1.527971923e-01f,-9.882575870e-01f,1.497645378e-01f,-9.887216687e-01f,
  1.467304677e-01f,-9.891765118e-01f,1.436950266e-01f,-9.896219969e-01f,1.406582445e-01f,-9.900581837e-01f,1.376201212e-01f,-9.904850721e-01f,
  1.345807016e-01f,-9.909026623e-01f,1.315400302e-01f,-9.913108349e-01f,1.284981072e-01f,-9.917097688e-01f,1.254549772e-01f,-9.920992851e-01f,
  1.224106774e-01f,-9.924795628e-01f,1.193652153e-01f,-9.928504229e-01f,1.163186282e-01f,-9.932119250e-01f,1.132709533e-01f,-9.935641289e-01f,
  1.102222055e-01f,-9.939069748e-01f,1.071724221e-01f,-9.942404628e-01f,1.041216329e-01f,-9.945645928e-01f,1.010698602e-01f,-9.948793054e-01f,
  9.801714122e-02f,-9.951847196e-01f,9.496349841e-02f,-9.954807758e-01f,9.190895408e-02f,-9.957674146e-01f,8.885355294e-02f,-9.960446954e-01f,
  8.579730988e-02f,-9.963126183e-01f,8.274026215e-02f,-9.965711236e-01f,7.968243957e-02f,-9.968202710e-01f,7.662386447e-02f,-9.970600605e-01f,
  7.356456667e-02f,-9.972904325e-01f,7.050457597e-02f,-9.975114465e-01f,6.744392216e-02f,-9.977230430e-01f,6.438262761e-02f,-9.979252815e-01f,
  6.132073700e-02f,-9.981181026e-01f,5.825826526e-02f,-9.983015656e-01f,5.519524589e-02f,-9.984755516e-01f,5.213170499e-02f,-9.986402392e-01f,
  4.906767607e-02f,-9.987954497e-01f,4.600318149e-02f,-9.989413023e-01f,4.293825850e-02f,-9.990777373e-01f,3.987292573e-02f,-9.992047548e-01f,
  3.680722415e-02f,-9.993223548e-01f,3.374117240e-02f,-9.994305968e-01f,3.067480400e-02f,-9.995294213e-01f,2.760814503e-02f,-9.996188283e-01f,
  2.454122901e-02f,-9.996988177e-01f,2.147408016e-02f,-9.997693896e-01f,1.840673015e-02f,-9.998306036e-01f,1.533920597e-02f,-9.998823404e-01f,
  1.227153838e-02f,-9.999247193e-01f,9.203754365e-03f,-9.999576211e-01f,6.135884672e-03f,-9.999811649e-01f,3.067956772e-03f,-9.999952912e-01f,
  6.123234263e-17f,-1.000000000e+00f,-3.067956772e-03f,-9.999952912e-01f,-6.135884672e-03f,-9.999811649e-01f,-9.203754365e-03f,-9.999576211e-01f,
  -1.227153838e-02f,-9.999247193e-01f,-1.533920597e-02f,-9.998823404e-01f,-1.840673015e-02f,-9.998306036e-01f,-2.147408016e-02f,-9.997693896e-01f,
  -2.454122901e-02f,-9.996988177e-01f,-2.760814503e-02f,-9.996188283e-01f,-3.067480400e-02f,-9.995294213e-01f,-3.374117240e-02f,-9.994305968e-01f,
  -3.680722415e-02f,-9.993223548e-01f,-3.987292573e-02f,-9.992047548e-01f,-4.293825850e-02f,-9.990777373e-01f,-4.600318149e-02f,-9.989413023e-01f,
  -4.906767607e-02f,-9.987954497e-01f,-5.213170499e-02f,-9.986402392e-01f,-5.519524589e-02f,-9.984755516e-01f,-5.825826526e-02f,-9.983015656e-01f,
  -6.132073700e-02f,-9.981181026e-01f,-6.438262761e-02f,-9.979252815e-01f,-6.744392216e-02f,-9.977230430e-01f,-7.050457597e-02f,-9.975114465e-01f,
  -7.356456667e-02f,-9.972904325e-01f,-7.662386447e-02f,-9.970600605e-01f,-7.968243957e-02f,-9.968202710e-01f,-8.274026215e-02f,-9.965711236e-01f,
  -8.579730988e-02f,-9.963126183e-01f,-8.885355294e-02f,-9.960446954e-01f,-9.190895408e-02f,-9.957674146e-01f,-9.496349841e-02f,-9.954807758e-01f,
  -9.801714122e-02f,-9.951847196e-01f,-1.010698602e-01f,-9.948793054e-01f,-1.041216329e-01f,-9.945645928e-01f,-1.071724221e-01f,-9.942404628e-01f,
  -1.102222055e-01f,-9.939069748e-01f,-1.132709533e-01f,-9.935641289e-01f,-1.163186282e-01f,-9.932119250e-01f,-1.193652153e-01f,-9.928504229e-01f,
  -1.224106774e-01f,-9.924795628e-01f,-1.254549772e-01f,-9.920992851e-01f,-1.284981072e-01f,-9.917097688e-01f,-1.315400302e-01f,-9.913108349e-01f,
  -1.345807016e-01f,-9.909026623e-01f,-1.376201212e-01f,-9.904850721e-01f,-1.406582445e-01f,-9.900581837e-01f,-1.436950266e-01f,-9.896219969e-01f,
  -1.467304677e-01f,-9.891765118e-01f,-1.497645378e-01f,-9.887216687e-01f,-1.527971923e-01f,-9.882575870e-01f,-1.558284014e-01f,-9.877841473e-01f,
  -1.588581502e-01f,-9.873014092e-01f,-1.618863940e-01f,-9.868093729e-01f,-1.649131179e-01f,-9.863080978e-01f,-1.679382920e-01f,-9.857975245e-01f,
  -1.709618866e-01f,-9.852776527e-01f,-1.739838719e-01f,-9.847484827e-01f,-1.770042181e-01f,-9.842100739e-01f,-1.800228953e-01f,-9.836624265e-01f,
  -1.830398887e-01f,-9.831054807e-01f,-1.860551536e-01f,-9.825392962e-01f,-1.890686601e-01f,-9.819638729e-01f,-1.920803934e-01f,-9.813792109e-01f,
  -1.950903237e-01f,-9.807852507e-01f,-1.980984062e-01f,-9.801821113e-01f,-2.011046410e-01f,-9.795697927e-01f,-2.041089684e-01f,-9.789481759e-01f,
  -2.071113735e-01f,-9.783173800e-01f,-2.101118416e-01f,-9.776773453e-01f,-2.131103128e-01f,-9.770281315e-01f,-2.161068022e-01f,-9.763697386e-01f,
  -2.191012353e-01f,-9.757021070e-01f,-2.220936269e-01f,-9.750253558e-01f,-2.250839174e-01f,-9.743393660e-01f,-2.280720770e-01f,-9.736442566e-01f,
  -2.310581058e-01f,-9.729399681e-01f,-2.340419590e-01f,-9.722265005e-01f,-2.370236069e-01f,-9.715039134e-01f,-2.400030196e-01f,-9.707721472e-01f,
  -2.429801822e-01f,-9.700312614e-01f,-2.459550500e-01f,-9.692812562e-01f,-2.489276081e-01f,-9.685220718e-01f,-2.518978119e-01f,-9.677538276e-01f,
  -2.548656464e-01f,-9.669764638e-01f,-2.578310966e-01f,-9.661899805e-01f,-2.607941031e-01f,-9.653944373e-01f,-2.637546659e-01f,-9.645897746e-01f,
  -2.667127550e-01f,-9.637760520e-01f,-2.696683109e-01f,-9.629532695e-01f,-2.726213634e-01f,-9.621214271e-01f,-2.755718231e-01f,-9.612804651e-01f,
  -2.785196900e-01f,-9.604305029e-01f,-2.814649343e-01f,-9.595715404e-01f,-2.844075263e-01f,-9.587034583e-01f,-2.873474658e-01f,-9.578264356e-01f,
  -2.902846634e-01f,-9.569403529e-01f,-2.932191491e-01f,-9.560452700e-01f,-2.961508930e-01f,-9.551411867e-01f,-2.990798354e-01f,-9.542281032e-01f,
  -3.020059466e-01f,-9.533060193e-01f,-3.049292266e-01f,-9.523749948e-01f,-3.078496456e-01f,-9.514350295e-01f,-3.107671440e-01f,-9.504860640e-01f,
  -3.136817515e-01f,-9.495281577e-01f,-3.165933788e-01f,-9.485613704e-01f,-3.195020258e-01f,-9.475855827e-01f,-3.224076927e-01f,-9.466009140e-01f,
  -3.253102899e-01f,-9.456073046e-01f,-3.282098472e-01f,-9.446048141e-01f,-3.311063051e-01f,-9.435934424e-01f,-3.339996636e-01f,-9.425731897e-01f,
  -3.368898630e-01f,-9.415440559e-01f,-3.397768736e-01f,-9.405060410e-01f,-3.426607251e-01f,-9.394592047e-01f,-3.455413282e-01f,-9.384035468e-01f,
  -3.484186828e-01f,-9.373390079e-01f,-3.512927592e-01f,-9.362656474e-01f,-3.541635275e-01f,-9.351835251e-01f,-3.570309579e-01f,-9.340925217e-01f,
  -3.598950505e-01f,-9.329928160e-01f,-3.627557158e-01f,-9.318842888e-01f,-3.656129837e-01f,-9.307669401e-01f,-3.684668243e-01f,-9.296408892e-01f,
  -3.713172078e-01f,-9.285060763e-01f,-3.741640747e-01f,-9.273625016e-01f,-3.770074248e-01f,-9.262102246e-01f,-3.798471987e-01f,-9.250492454e-01f,
  -3.826834261e-01f,-9.238795042e-01f,-3.855160475e-01f,-9.227011204e-01f,-3.883450329e-01f,-9.215140343e-01f,-3.911703825e-01f,-9.203183055e-01f,
  -3.939920366e-01f,-9.191138744e-01f,-3.968099952e-01f,-9.179008007e-01f,-3.996241987e-01f,-9.166790843e-01f,-4.024346471e-01f,-9.154487252e-01f,
  -4.052413106e-01f,-9.142097831e-01f,-4.080441594e-01f,-9.129621983e-01f,-4.108431637e-01f,-9.117060304e-01f,-4.136383235e-01f,-9.104412794e-01f,
  -4.164295495e-01f,-9.091680050e-01f,-4.192169011e-01f,-9.078860879e-01f,-4.220002592e-01f,-9.065957069e-01f,-4.247796834e-01f,-9.052967429e-01f,
  -4.275550842e-01f,-9.039893150e-01f,-4.303264916e-01f,-9.026733041e-01f,-4.330938160e-01f,-9.013488293e-01f,-4.358570874e-01f,-9.000158906e-01f,
  -4.386162460e-01f,-8.986744881e-01f,-4.413712621e-01f,-8.973245621e-01f,-4.441221356e-01f,-8.959662318e-01f,-4.468688369e-01f,-8.945994973e-01f,
  -4.496113360e-01f,-8.932242990e-01f,-4.523495734e-01f,-8.918406963e-01f,-4.550835788e-01f,-8.904487491e-01f,-4.578132927e-01f,-8.890483379e-01f,
  -4.605387151e-01f,-8.876396418e-01f,-4.632597864e-01f,-8.862225413e-01f,-4.659765065e-01f,-8.847970963e-01f,-4.686888158e-01f,-8.833633661e-01f,
  -4.713967443e-01f,-8.819212914e-01f,-4.741002023e-01f,-8.804708719e-01f,-4.767992198e-01f,-8.790122271e-01f,-4.794937670e-01f,-8.775452971e-01f,
  -4.821837842e-01f,-8.760700822e-01f,-4.848692417e-01f,-8.745866418e-01f,-4.875501692e-01f,-8.730949759e-01f,-4.902264774e-01f,-8.715950847e-01f,
  -4.928981960e-01f,-8.700869679e-01f,-4.955652654e-01f,-8.685706854e-01f,-4.982276559e-01f,-8.670462370e-01f,-5.008853674e-01f,-8.655136228e-01f,
  -5.035383701e-01f,-8.639728427e-01f,-5.061866641e-01f,-8.624239564e-01f,-5.088301301e-01f,-8.608669639e-01f,-5.114688277e-01f,-8.593018055e-01f,
  -5.141027570e-01f,-8.577286005e-01f,-5.167317986e-01f,-8.561473489e-01f,-5.193560123e-01f,-8.545579910e-01f,-5.219752789e-01f,-8.529605865e-01f,
  -5.245896578e-01f,-8.513551950e-01f,-5.271991491e-01f,-8.497417569e-01f,-5.298036337e-01f,-8.481203318e-01f,-5.324031115e-01f,-8.464909196e-01f,
  -5.349976420e-01f,-8.448535800e-01f,-5.375870466e-01f,-8.432082534e-01f,-5.401714444e-01f,-8.415549994e-01f,-5.427507758e-01f,-8.398938179e-01f,
  -5.453249812e-01f,-8.382247090e-01f,-5.478940606e-01f,-8.365477324e-01f,-5.504579544e-01f,-8.348628879e-01f,-5.530167222e-01f,-8.331701756e-01f,
  -5.555702448e-01f,-8.314695954e-01f,-5.581185222e-01f,-8.297612071e-01f,-5.606615543e-01f,-8.280450702e-01f,-5.631993413e-01f,-8.263210654e-01f,
  -5.657318234e-01f,-8.245893121e-01f,-5.682589412e-01f,-8.228498101e-01f,-5.707807541e-01f,-8.211025000e-01f,-5.732971430e-01f,-8.193475008e-01f,
  -5.758081675e-01f,-8.175848126e-01f,-5.783137679e-01f,-8.158144355e-01f,-5.808139443e-01f,-8.140363097e-01f,-5.833086371e-01f,-8.122506142e-01f,
  -5.857978463e-01f,-8.104571700e-01f,-5.882815719e-01f,-8.086561561e-01f,-5.907596946e-01f,-8.068475723e-01f,-5.932322741e-01f,-8.050313592e-01f,
  -5.956993103e-01f,-8.032075167e-01f,-5.981606841e-01f,-8.013761640e-01f,-6.006164551e-01f,-7.995372415e-01f,-6.030666232e-01f,-7.976908684e-01f,
  -6.055110693e-01f,-7.958369255e-01f,-6.079497933e-01f,-7.939754725e-01f,-6.103827953e-01f,-7.921065688e-01f,-6.128100753e-01f,-7.902302146e-01f,
  -6.152315736e-01f,-7.883464098e-01f,-6.176472902e-01f,-7.864552140e-01f,-6.200572252e-01f,-7.845565677e-01f,-6.224612594e-01f,-7.826505899e-01f,
  -6.248595119e-01f,-7.807372212e-01f,-6.272518039e-01f,-7.788165212e-01f,-6.296382546e-01f,-7.768884897e-01f,-6.320187449e-01f,-7.749531269e-01f,
  -6.343932748e-01f,-7.730104327e-01f,-6.367618442e-01f,-7.710605264e-01f,-6.391244531e-01f,-7.691033483e-01f,-6.414810419e-01f,-7.671388984e-01f,
  -6.438315511e-01f,-7.651672363e-01f,-6.461760402e-01f,-7.631884217e-01f,-6.485143900e-01f,-7.612023950e-01f,-6.508466601e-01f,-7.592092156e-01f,
  -6.531728506e-01f,-7.572088242e-01f,-6.554928422e-01f,-7.552013993e-01f,-6.578066945e-01f,-7.531868219e-01f,-6.601143479e-01f,-7.511651516e-01f,
  -6.624158025e-01f,-7.491363883e-01f,-6.647109985e-01f,-7.471005917e-01f,-6.669999361e-01f,-7.450577617e-01f,-6.692826152e-01f,-7.430079579e-01f,
  -6.715589762e-01f,-7.409511209e-01f,-6.738290191e-01f,-7.388873100e-01f,-6.760926843e-01f,-7.368165851e-01f,-6.783500314e-01f,-7.347388864e-01f,
  -6.806010008e-01f,-7.326542735e-01f,-6.828455329e-01f,-7.305627465e-01f,-6.850836873e-01f,-7.284643650e-01f,-6.873153448e-01f,-7.263591290e-01f,
  -6.895405650e-01f,-7.242470980e-01f,-6.917592287e-01f,-7.221282125e-01f,-6.939714551e-01f,-7.200025320e-01f,-6.961771250e-01f,-7.178700566e-01f,
  -6.983762383e-01f,-7.157308459e-01f,-7.005687952e-01f,-7.135848403e-01f,-7.027547359e-01f,-7.114322186e-01f,-7.049340606e-01f,-7.092728019e-01f,
  -7.071067691e-01f,-7.071067691e-01f,-7.092728019e-01f,-7.049340606e-01f,-7.114322186e-01f,-7.027547359e-01f,-7.135848403e-01f,-7.005687952e-01f,
  -7.157308459e-01f,-6.983762383e-01f,-7.178700566e-01f,-6.961771250e-01f,-7.200025320e-01f,-6.939714551e-01f,-7.221282125e-01f,-6.917592287e-01f,
  -7.242470980e-01f,-6.895405650e-01f,-7.263591290e-01f,-6.873153448e-01f,-7.284643650e-01f,-6.850836873e-01f,-7.305627465e-01f,-6.828455329e-01f,
  -7.326542735e-01f,-6.806010008e-01f,-7.347388864e-01f,-6.783500314e-01f,-7.368165851e-01f,-6.760926843e-01f,-7.388873100e-01f,-6.738290191e-01f,
  -7.409511209e-01f,-6.715589762e-01f,-7.430079579e-01f,-6.692826152e-01f,-7.450577617e-01f,-6.669999361e-01f,-7.471005917e-01f,-6.647109985e-01f,
  -7.491363883e-01f,-6.624158025e-01f,-7.511651516e-01f,-6.601143479e-01f,-7.531868219e-01f,-6.578066945e-01f,-7.552013993e-01f,-6.554928422e-01f,
  -7.572088242e-01f,-6.531728506e-01f,-7.592092156e-01f,-6.508466601e-01f,-7.612023950e-01f,-6.485143900e-01f,-7.631884217e-01f,-6.461760402e-01f,
  -7.651672363e-01f,-6.438315511e-01f,-7.671388984e-01f,-6.414810419e-01f,-7.691033483e-01f,-6.391244531e-01f,-7.710605264e-01f,-6.367618442e-01f,
  -7.730104327e-01f,-6.343932748e-01f,-7.749531269e-01f,-6.320187449e-01f,-7.768884897e-01f,-6.296382546e-01f,-7.788165212e-01f,-6.272518039e-01f,
  -7.807372212e-01f,-6.248595119e-01f,-7.826505899e-01f,-6.224612594e-01f,-7.845565677e-01f,-6.200572252e-01f,-7.864552140e-01f,-6.176472902e-01f,
  -7.883464098e-01f,-6.152315736e-01f,-7.902302146e-01f,-6.128100753e-01f,-7.921065688e-01f,-6.103827953e-01f,-7.939754725e-01f,-6.079497933e-01f,
  -7.958369255e-01f,-6.055110693e-01f,-7.976908684e-01f,-6.030666232e-01f,-7.995372415e-01f,-6.006164551e-01f,-8.013761640e-01f,-5.981606841e-01f,
  -8.032075167e-01f,-5.956993103e-01f,-8.050313592e-01f,-5.932322741e-01f,-8.068475723e-01f,-5.907596946e-01f,-8.086561561e-01f,-5.882815719e-01f,
  -8.104571700e-01f,-5.857978463e-01f,-8.122506142e-01f,-5.833086371e-01f,-8.140363097e-01f,-5.808139443e-01f,-8.158144355e-01f,-5.783137679e-01f,
  -8.175848126e-01f,-5.758081675e-01f,-8.193475008e-01f,-5.732971430e-01f,-8.211025000e-01f,-5.707807541e-01f,-8.228498101e-01f,-5.682589412e-01f,
  -8.245893121e-01f,-5.657318234e-01f,-8.263210654e-01f,-5.631993413e-01f,-8.280450702e-01f,-5.606615543e-01f,-8.297612071e-01f,-5.581185222e-01f,
  -8.314695954e-01f,-5.555702448e-01f,-8.331701756e-01f,-5.530167222e-01f,-8.348628879e-01f,-5.504579544e-01f,-8.365477324e-01f,-5.478940606e-01f,
  -8.382247090e-01f,-5.453249812e-01f,-8.398938179e-01f,-5.427507758e-01f,-8.415549994e-01f,-5.401714444e-01f,-8.432082534e-01f,-5.375870466e-01f,
  -8.448535800e-01f,-5.349976420e-01f,-8.464909196e-01f,-5.324031115e-01f,-8.481203318e-01f,-5.298036337e-01f,-8.497417569e-01f,-5.271991491e-01f,
  -8.513551950e-01f,-5.245896578e-01f,-8.529605865e-01f,-5.219752789e-01f,-8.545579910e-01f,-5.193560123e-01f,-8.561473489e-01f,-5.167317986e-01f,
  -8.577286005e-01f,-5.141027570e-01f,-8.593018055e-01f,-5.114688277e-01f,-8.608669639e-01f,-5.088301301e-01f,-8.624239564e-01f,-5.061866641e-01f,
  -8.639728427e-01f,-5.035383701e-01f,-8.655136228e-01f,-5.008853674e-01f,-8.670462370e-01f,-4.982276559e-01f,-8.685706854e-01f,-4.955652654e-01f,
  -8.700869679e-01f,-4.928981960e-01f,-8.715950847e-01f,-4.902264774e-01f,-8.730949759e-01f,-4.875501692e-01f,-8.745866418e-01f,-4.848692417e-01f,
  -8.760700822e-01f,-4.821837842e-01f,-8.775452971e-01f,-4.794937670e-01f,-8.790122271e-01f,-4.767992198e-01f,-8.804708719e-01f,-4.741002023e-01f,
  -8.819212914e-01f,-4.713967443e-01f,-8.833633661e-01f,-4.686888158e-01f,-8.847970963e-01f,-4.659765065e-01f,-8.862225413e-01f,-4.632597864e-01f,
  -8.876396418e-01f,-4.605387151e-01f,-8.890483379e-01f,-4.578132927e-01f,-8.904487491e-01f,-4.550835788e-01f,-8.918406963e-01f,-4.523495734e-01f,
  -8.932242990e-01f,-4.496113360e-01f,-8.945994973e-01f,-4.468688369e-01f,-8.959662318e-01f,-4.441221356e-01f,-8.973245621e-01f,-4.413712621e-01f,
  -8.986744881e-01f,-4.386162460e-01f,-9.000158906e-01f,-4.358570874e-01f,-9.013488293e-01f,-4.330938160e-01f,-9.026733041e-01f,-4.303264916e-01f,
  -9.039893150e-01f,-4.275550842e-01f,-9.052967429e-01f,-4.247796834e-01f,-9.065957069e-01f,-4.220002592e-01f,-9.078860879e-01f,-4.192169011e-01f,
  -9.091680050e-01f,-4.164295495e-01f,-9.104412794e-01f,-4.136383235e-01f,-9.117060304e-01f,-4.108431637e-01f,-9.129621983e-01f,-4.080441594e-01f,
  -9.142097831e-01f,-4.052413106e-01f,-9.154487252e-01f,-4.024346471e-01f,-9.166790843e-01f,-3.996241987e-01f,-9.179008007e-01f,-3.968099952e-01f,
  -9.191138744e-01f,-3.939920366e-01f,-9.203183055e-01f,-3.911703825e-01f,-9.215140343e-01f,-3.883450329e-01f,-9.227011204e-01f,-3.855160475e-01f,
  -9.238795042e-01f,-3.826834261e-01f,-9.250492454e-01f,-3.798471987e-01f,-9.262102246e-01f,-3.770074248e-01f,-9.273625016e-01f,-3.741640747e-01f,
  -9.285060763e-01f,-3.713172078e-01f,-9.296408892e-01f,-3.684668243e-01f,-9.307669401e-01f,-3.656129837e-01f,-9.318842888e-01f,-3.627557158e-01f,
  -9.329928160e-01f,-3.598950505e-01f,-9.340925217e-01f,-3.570309579e-01f,-9.351835251e-01f,-3.541635275e-01f,-9.362656474e-01f,-3.512927592e-01f,
  -9.373390079e-01f,-3.484186828e-01f,-9.384035468e-01f,-3.455413282e-01f,-9.394592047e-01f,-3.426607251e-01f,-9.405060410e-01f,-3.397768736e-01f,
  -9.415440559e-01f,-3.368898630e-01f,-9.425731897e-01f,-3.339996636e-01f,-9.435934424e-01f,-3.311063051e-01f,-9.446048141e-01f,-3.282098472e-01f,
  -9.456073046e-01f,-3.253102899e-01f,-9.466009140e-01f,-3.224076927e-01f,-9.475855827e-01f,-3.195020258e-01f,-9.485613704e-01f,-3.165933788e-01f,
  -9.495281577e-01f,-3.136817515e-01f,-9.504860640e-01f,-3.107671440e-01f,-9.514350295e-01f,-3.078496456e-01f,-9.523749948e-01f,-3.049292266e-01f,
  -9.533060193e-01f,-3.020059466e-01f,-9.542281032e-01f,-2.990798354e-01f,-9.551411867e-01f,-2.961508930e-01f,-9.560452700e-01f,-2.932191491e-01f,
  -9.569403529e-01f,-2.902846634e-01f,-9.578264356e-01f,-2.873474658e-01f,-9.587034583e-01f,-2.844075263e-01f,-9.595715404e-01f,-2.814649343e-01f,
  -9.604305029e-01f,-2.785196900e-01f,-9.612804651e-01f,-2.755718231e-01f,-9.621214271e-01f,-2.726213634e-01f,-9.629532695e-01f,-2.696683109e-01f,
  -9.637760520e-01f,-2.667127550e-01f,-9.645897746e-01f,-2.637546659e-01f,-9.653944373e-01f,-2.607941031e-01f,-9.661899805e-01f,-2.578310966e-01f,
  -9.669764638e-01f,-2.548656464e-01f,-9.677538276e-01f,-2.518978119e-01f,-9.685220718e-01f,-2.489276081e-01f,-9.692812562e-01f,-2.459550500e-01f,
  -9.700312614e-01f,-2.429801822e-01f,-9.707721472e-01f,-2.400030196e-01f,-9.715039134e-01f,-2.370236069e-01f,-9.722265005e-01f,-2.340419590e-01f,
  -9.729399681e-01f,-2.310581058e-01f,-9.736442566e-01f,-2.280720770e-01f,-9.743393660e-01f,-2.250839174e-01f,-9.750253558e-01f,-2.220936269e-01f,
  -9.757021070e-01f,-2.191012353e-01f,-9.763697386e-01f,-2.161068022e-01f,-9.770281315e-01f,-2.131103128e-01f,-9.776773453e-01f,-2.101118416e-01f,
  -9.783173800e-01f,-2.071113735e-01f,-9.789481759e-01f,-2.041089684e-01f,-9.795697927e-01f,-2.011046410e-01f,-9.801821113e-01f,-1.980984062e-01f,
  -9.807852507e-01f,-1.950903237e-01f,-9.813792109e-01f,-1.920803934e-01f,-9.819638729e-01f,-1.890686601e-01f,-9.825392962e-01f,-1.860551536e-01f,
  -9.831054807e-01f,-1.830398887e-01f,-9.836624265e-01f,-1.800228953e-01f,-9.842100739e-01f,-1.770042181e-01f,-9.847484827e-01f,-1.739838719e-01f,
  -9.852776527e-01f,-1.709618866e-01f,-9.857975245e-01f,-1.679382920e-01f,-9.863080978e-01f,-1.649131179e-01f,-9.868093729e-01f,-1.618863940e-01f,
  -9.873014092e-01f,-1.588581502e-01f,-9.877841473e-01f,-1.558284014e-01f,-9.882575870e-01f,-1.527971923e-01f,-9.887216687e-01f,-1.497645378e-01f,
  -9.891765118e-01f,-1.467304677e-01f,-9.896219969e-01f,-1.436950266e-01f,-9.900581837e-01f,-1.406582445e-01f,-9.904850721e-01f,-1.376201212e-01f,
  -9.909026623e-01f,-1.345807016e-01f,-9.913108349e-01f,-1.315400302e-01f,-9.917097688e-01f,-1.284981072e-01f,-9.920992851e-01f,-1.254549772e-01f,
  -9.924795628e-01f,-1.224106774e-01f,-9.928504229e-01f,-1.193652153e-01f,-9.932119250e-01f,-1.163186282e-01f,-9.935641289e-01f,-1.132709533e-01f,
  -9.939069748e-01f,-1.102222055e-01f,-9.942404628e-01f,-1.071724221e-01f,-9.945645928e-01f,-1.041216329e-01f,-9.948793054e-01f,-1.010698602e-01f,
  -9.951847196e-01f,-9.801714122e-02f,-9.954807758e-01f,-9.496349841e-02f,-9.957674146e-01f,-9.190895408e-02f,-9.960446954e-01f,-8.885355294e-02f,
  -9.963126183e-01f,-8.579730988e-02f,-9.965711236e-01f,-8.274026215e-02f,-9.968202710e-01f,-7.968243957e-02f,-9.970600605e-01f,-7.662386447e-02f,
  -9.972904325e-01f,-7.356456667e-02f,-9.975114465e-01f,-7.050457597e-02f,-9.977230430e-01f,-6.744392216e-02f,-9.979252815e-01f,-6.438262761e-02f,
  -9.981181026e-01f,-6.132073700e-02f,-9.983015656e-01f,-5.825826526e-02f,-9.984755516e-01f,-5.519524589e-02f,-9.986402392e-01f,-5.213170499e-02f,
  -9.987954497e-01f,-4.906767607e-02f,-9.989413023e-01f,-4.600318149e-02f,-9.990777373e-01f,-4.293825850e-02f,-9.992047548e-01f,-3.987292573e-02f,
  -9.993223548e-01f,-3.680722415e-02f,-9.994305968e-01f,-3.374117240e-02f,-9.995294213e-01f,-3.067480400e-02f,-9.996188283e-01f,-2.760814503e-02f,
  -9.996988177e-01f,-2.454122901e-02f,-9.997693896e-01f,-2.147408016e-02f,-9.998306036e-01f,-1.840673015e-02f,-9.998823404e-01f,-1.533920597e-02f,
  -9.999247193e-01f,-1.227153838e-02f,-9.999576211e-01f,-9.203754365e-03f,-9.999811649e-01f,-6.135884672e-03f,-9.999952912e-01f,-3.067956772e-03f,
  -1.000000000e+00f,-1.224646853e-16f,-9.999952912e-01f,3.067956772e-03f,-9.999811649e-01f,6.135884672e-03f,-9.999576211e-01f,9.203754365e-03f,
  -9.999247193e-01f,1.227153838e-02f,-9.998823404e-01f,1.533920597e-02f,-9.998306036e-01f,1.840673015e-02f,-9.997693896e-01f,2.147408016e-02f,
  -9.996988177e-01f,2.454122901e-02f,-9.996188283e-01f,2.760814503e-02f,-9.995294213e-01f,3.067480400e-02f,-9.994305968e-01f,3.374117240e-02f,
  -9.993223548e-01f,3.680722415e-02f,-9.992047548e-01f,3.987292573e-02f,-9.990777373e-01f,4.293825850e-02f,-9.989413023e-01f,4.600318149e-02f,
  -9.987954497e-01f,4.906767607e-02f,-9.986402392e-01f,5.213170499e-02f,-9.984755516e-01f,5.519524589e-02f,-9.983015656e-01f,5.825826526e-02f,
  -9.981181026e-01f,6.132073700e-02f,-9.979252815e-01f,6.438262761e-02f,-9.977230430e-01f,6.744392216e-02f,-9.975114465e-01f,7.050457597e-02f,
  -9.972904325e-01f,7.356456667e-02f,-9.970600605e-01f,7.662386447e-02f,-9.968202710e-01f,7.968243957e-02f,-9.965711236e-01f,8.274026215e-02f,
  -9.963126183e-01f,8.579730988e-02f,-9.960446954e-01f,8.885355294e-02f,-9.957674146e-01f,9.190895408e-02f,-9.954807758e-01f,9.496349841e-02f,
  -9.951847196e-01f,9.801714122e-02f,-9.948793054e-01f,1.010698602e-01f,-9.945645928e-01f,1.041216329e-01f,-9.942404628e-01f,1.071724221e-01f,
  -9.939069748e-01f,1.102222055e-01f,-9.935641289e-01f,1.132709533e-01f,-9.932119250e-01f,1.163186282e-01f,-9.928504229e-01f,1.193652153e-01f,
  -9.924795628e-01f,1.224106774e-01f,-9.920992851e-01f,1.254549772e-01f,-9.917097688e-01f,1.284981072e-01f,-9.913108349e-01f,1.315400302e-01f,
  -9.909026623e-01f,1.345807016e-01f,-9.904850721e-01f,1.376201212e-01f,-9.900581837e-01f,1.406582445e-01f,-9.896219969e-01f,1.436950266e-01f,
  -9.891765118e-01f,1.467304677e-01f,-9.887216687e-01f,1.497645378e-01f,-9.882575870e-01f,1.527971923e-01f,-9.877841473e-01f,1.558284014e-01f,
  -9.873014092e-01f,1.588581502e-01f,-9.868093729e-01f,1.618863940e-01f,-9.863080978e-01f,1.649131179e-01f,-9.857975245e-01f,1.679382920e-01f,
  -9.852776527e-01f,1.709618866e-01f,-9.847484827e-01f,1.739838719e-01f,-9.842100739e-01f,1.770042181e-01f,-9.836624265e-01f,1.800228953e-01f,
  -9.831054807e-01f,1.830398887e-01f,-9.825392962e-01f,1.860551536e-01f,-9.819638729e-01f,1.890686601e-01f,-9.813792109e-01f,1.920803934e-01f,
  -9.807852507e-01f,1.950903237e-01f,-9.801821113e-01f,1.980984062e-01f,-9.795697927e-01f,2.011046410e-01f,-9.789481759e-01f,2.041089684e-01f,
  -9.783173800e-01f,2.071113735e-01f,-9.776773453e-01f,2.101118416e-01f,-9.770281315e-01f,2.131103128e-01f,-9.763697386e-01f,2.161068022e-01f,
  -9.757021070e-01f,2.191012353e-01f,-9.750253558e-01f,2.220936269e-01f,-9.743393660e-01f,2.250839174e-01f,-9.736442566e-01f,2.280720770e-01f,
  -9.729399681e-01f,2.310581058e-01f,-9.722265005e-01f,2.340419590e-01f,-9.715039134e-01f,2.370236069e-01f,-9.707721472e-01f,2.400030196e-01f,
  -9.700312614e-01f,2.429801822e-01f,-9.692812562e-01f,2.459550500e-01f,-9.685220718e-01f,2.489276081e-01f,-9.677538276e-01f,2.518978119e-01f,
  -9.669764638e-01f,2.548656464e-01f,-9.661899805e-01f,2.578310966e-01f,-9.653944373e-01f,2.607941031e-01f,-9.645897746e-01f,2.637546659e-01f,
  -9.637760520e-01f,2.667127550e-01f,-9.629532695e-01f,2.696683109e-01f,-9.621214271e-01f,2.726213634e-01f,-9.612804651e-01f,2.755718231e-01f,
  -9.604305029e-01f,2.785196900e-01f,-9.595715404e-01f,2.814649343e-01f,-9.587034583e-01f,2.844075263e-01f,-9.578264356e-01f,2.873474658e-01f,
  -9.569403529e-01f,2.902846634e-01f,-9.560452700e-01f,2.932191491e-01f,-9.551411867e-01f,2.961508930e-01f,-9.542281032e-01f,2.990798354e-01f,
  -9.533060193e-01f,3.020059466e-01f,-9.523749948e-01f,3.049292266e-01f,-9.514350295e-01f,3.078496456e-01f,-9.504860640e-01f,3.107671440e-01f,
  -9.495281577e-01f,3.136817515e-01f,-9.485613704e-01f,3.165933788e-01f,-9.475855827e-01f,3.195020258e-01f,-9.466009140e-01f,3.224076927e-01f,
  -9.456073046e-01f,3.253102899e-01f,-9.446048141e-01f,3.282098472e-01f,-9.435934424e-01f,3.311063051e-01f,-9.425731897e-01f,3.339996636e-01f,
  -9.415440559e-01f,3.368898630e-01f,-9.405060410e-01f,3.397768736e-01f,-9.394592047e-01f,3.426607251e-01f,-9.384035468e-01f,3.455413282e-01f,
  -9.373390079e-01f,3.484186828e-01f,-9.362656474e-01f,3.512927592e-01f,-9.351835251e-01f,3.541635275e-01f,-9.340925217e-01f,3.570309579e-01f,
  -9.329928160e-01f,3.598950505e-01f,-9.318842888e-01f,3.627557158e-01f,-9.307669401e-01f,3.656129837e-01f,-9.296408892e-01f,3.684668243e-01f,
  -9.285060763e-01f,3.713172078e-01f,-9.273625016e-01f,3.741640747e-01f,-9.262102246e-01f,3.770074248e-01f,-9.250492454e-01f,3.798471987e-01f,
  -9.238795042e-01f,3.826834261e-01f,-9.227011204e-01f,3.855160475e-01f,-9.215140343e-01f,3.883450329e-01f,-9.203183055e-01f,3.911703825e-01f,
  -9.191138744e-01f,3.939920366e-01f,-9.179008007e-01f,3.968099952e-01f,-9.166790843e-01f,3.996241987e-01f,-9.154487252e-01f,4.024346471e-01f,
  -9.142097831e-01f,4.052413106e-01f,-9.129621983e-01f,4.080441594e-01f,-9.117060304e-01f,4.108431637e-01f,-9.104412794e-01f,4.136383235e-01f,
  -9.091680050e-01f,4.164295495e-01f,-9.078860879e-01f,4.192169011e-01f,-9.065957069e-01f,4.220002592e-01f,-9.052967429e-01f,4.247796834e-01f,
  -9.039893150e-01f,4.275550842e-01f,-9.026733041e-01f,4.303264916e-01f,-9.013488293e-01f,4.330938160e-01f,-9.000158906e-01f,4.358570874e-01f,
  -8.986744881e-01f,4.386162460e-01f,-8.973245621e-01f,4.413712621e-01f,-8.959662318e-01f,4.441221356e-01f,-8.945994973e-01f,4.468688369e-01f,
  -8.932242990e-01f,4.496113360e-01f,-8.918406963e-01f,4.523495734e-01f,-8.904487491e-01f,4.550835788e-01f,-8.890483379e-01f,4.578132927e-01f,
  -8.876396418e-01f,4.605387151e-01f,-8.862225413e-01f,4.632597864e-01f,-8.847970963e-01f,4.659765065e-01f,-8.833633661e-01f,4.686888158e-01f,
  -8.819212914e-01f,4.713967443e-01f,-8.804708719e-01f,4.741002023e-01f,-8.790122271e-01f,4.767992198e-01f,-8.775452971e-01f,4.794937670e-01f,
  -8.760700822e-01f,4.821837842e-01f,-8.745866418e-01f,4.848692417e-01f,-8.730949759e-01f,4.875501692e-01f,-8.715950847e-01f,4.902264774e-01f,
  -8.700869679e-01f,4.928981960e-01f,-8.685706854e-01f,4.955652654e-01f,-8.670462370e-01f,4.982276559e-01f,-8.655136228e-01f,5.008853674e-01f,
  -8.639728427e-01f,5.035383701e-01f,-8.624239564e-01f,5.061866641e-01f,-8.608669639e-01f,5.088301301e-01f,-8.593018055e-01f,5.114688277e-01f,
  -8.577286005e-01f,5.141027570e-01f,-8.561473489e-01f,5.167317986e-01f,-8.545579910e-01f,5.193560123e-01f,-8.529605865e-01f,5.219752789e-01f,
  -8.513551950e-01f,5.245896578e-01f,-8.497417569e-01f,5.271991491e-01f,-8.481203318e-01f,5.298036337e-01f,-8.464909196e-01f,5.324031115e-01f,
  -8.448535800e-01f,5.349976420e-01f,-8.432082534e-01f,5.375870466e-01f,-8.415549994e-01f,5.401714444e-01f,-8.398938179e-01f,5.427507758e-01f,
  -8.382247090e-01f,5.453249812e-01f,-8.365477324e-01f,5.478940606e-01f,-8.348628879e-01f,5.504579544e-01f,-8.331701756e-01f,5.530167222e-01f,
  -8.314695954e-01f,5.555702448e-01f,-8.297612071e-01f,5.581185222e-01f,-8.280450702e-01f,5.606615543e-01f,-8.263210654e-01f,5.631993413e-01f,
  -8.245893121e-01f,5.657318234e-01f,-8.228498101e-01f,5.682589412e-01f,-8.211025000e-01f,5.707807541e-01f,-8.193475008e-01f,5.732971430e-01f,
  -8.175848126e-01f,5.758081675e-01f,-8.158144355e-01f,5.783137679e-01f,-8.140363097e-01f,5.808139443e-01f,-8.122506142e-01f,5.833086371e-01f,
  -8.104571700e-01f,5.857978463e-01f,-8.086561561e-01f,5.882815719e-01f,-8.068475723e-01f,5.907596946e-01f,-8.050313592e-01f,5.932322741e-01f,
  -8.032075167e-01f,5.956993103e-01f,-8.013761640e-01f,5.981606841e-01f,-7.995372415e-01f,6.006164551e-01f,-7.976908684e-01f,6.030666232e-01f,
  -7.958369255e-01f,6.055110693e-01f,-7.939754725e-01f,6.079497933e-01f,-7.921065688e-01f,6.103827953e-01f,-7.902302146e-01f,6.128100753e-01f,
  -7.883464098e-01f,6.152315736e-01f,-7.864552140e-01f,6.176472902e-01f,-7.845565677e-01f,6.200572252e-01f,-7.826505899e-01f,6.224612594e-01f,
  -7.807372212e-01f,6.248595119e-01f,-7.788165212e-01f,6.272518039e-01f,-7.768884897e-01f,6.296382546e-01f,-7.749531269e-01f,6.320187449e-01f,
  -7.730104327e-01f,6.343932748e-01f,-7.710605264e-01f,6.367618442e-01f,-7.691033483e-01f,6.391244531e-01f,-7.671388984e-01f,6.414810419e-01f,
  -7.651672363e-01f,6.438315511e-01f,-7.631884217e-01f,6.461760402e-01f,-7.612023950e-01f,6.485143900e-01f,-7.592092156e-01f,6.508466601e-01f,
  -7.572088242e-01f,6.531728506e-01f,-7.552013993e-01f,6.554928422e-01f,-7.531868219e-01f,6.578066945e-01f,-7.511651516e-01f,6.601143479e-01f,
  -7.491363883e-01f,6.624158025e-01f,-7.471005917e-01f,6.647109985e-01f,-7.450577617e-01f,6.669999361e-01f,-7.430079579e-01f,6.692826152e-01f,
  -7.409511209e-01f,6.715589762e-01f,-7.388873100e-01f,6.738290191e-01f,-7.368165851e-01f,6.760926843e-01f,-7.347388864e-01f,6.783500314e-01f,
  -7.326542735e-01f,6.806010008e-01f,-7.305627465e-01f,6.828455329e-01f,-7.284643650e-01f,6.850836873e-01f,-7.263591290e-01f,6.873153448e-01f,
  -7.242470980e-01f,6.895405650e-01f,-7.221282125e-01f,6.917592287e-01f,-7.200025320e-01f,6.939714551e-01f,-7.178700566e-01f,6.961771250e-01f,
  -7.157308459e-01f,6.983762383e-01f,-7.135848403e-01f,7.005687952e-01f,-7.114322186e-01f,7.027547359e-01f,-7.092728019e-01f,7.049340606e-01f,
  -7.071067691e-01f,7.071067691e-01f,-7.049340606e-01f,7.092728019e-01f,-7.027547359e-01f,7.114322186e-01f,-7.005687952e-01f,7.135848403e-01f,
  -6.983762383e-01f,7.157308459e-01f,-6.961771250e-01f,7.178700566e-01f,-6.939714551e-01f,7.200025320e-01f,-6.917592287e-01f,7.221282125e-01f,
  -6.895405650e-01f,7.242470980e-01f,-6.873153448e-01f,7.263591290e-01f,-6.850836873e-01f,7.284643650e-01f,-6.828455329e-01f,7.305627465e-01f,
  -6.806010008e-01f,7.326542735e-01f,-6.783500314e-01f,7.347388864e-01f,-6.760926843e-01f,7.368165851e-01f,-6.738290191e-01f,7.388873100e-01f,
  -6.715589762e-01f,7.409511209e-01f,-6.692826152e-01f,7.430079579e-01f,-6.669999361e-01f,7.450577617e-01f,-6.647109985e-01f,7.471005917e-01f,
  -6.624158025e-01f,7.491363883e-01f,-6.601143479e-01f,7.511651516e-01f,-6.578066945e-01f,7.531868219e-01f,-6.554928422e-01f,7.552013993e-01f,
  -6.531728506e-01f,7.572088242e-01f,-6.508466601e-01f,7.592092156e-01f,-6.485143900e-01f,7.612023950e-01f,-6.461760402e-01f,7.631884217e-01f,
  -6.438315511e-01f,7.651672363e-01f,-6.414810419e-01f,7.671388984e-01f,-6.391244531e-01f,7.691033483e-01f,-6.367618442e-01f,7.710605264e-01f,
  -6.343932748e-01f,7.730104327e-01f,-6.320187449e-01f,7.749531269e-01f,-6.296382546e-01f,7.768884897e-01f,-6.272518039e-01f,7.788165212e-01f,
  -6.248595119e-01f,7.807372212e-01f,-6.224612594e-01f,7.826505899e-01f,-6.200572252e-01f,7.845565677e-01f,-6.176472902e-01f,7.864552140e-01f,
  -6.152315736e-01f,7.883464098e-01f,-6.128100753e-01f,7.902302146e-01f,-6.103827953e-01f,7.921065688e-01f,-6.079497933e-01f,7.939754725e-01f,
  -6.055110693e-01f,7.958369255e-01f,-6.030666232e-01f,7.976908684e-01f,-6.006164551e-01f,7.995372415e-01f,-5.981606841e-01f,8.013761640e-01f,
  -5.956993103e-01f,8.032075167e-01f,-5.932322741e-01f,8.050313592e-01f,-5.907596946e-01f,8.068475723e-01f,-5.882815719e-01f,8.086561561e-01f,
  -5.857978463e-01f,8.104571700e-01f,-5.833086371e-01f,8.122506142e-01f,-5.808139443e-01f,8.140363097e-01f,-5.783137679e-01f,8.158144355e-01f,
  -5.758081675e-01f,8.175848126e-01f,-5.732971430e-01f,8.193475008e-01f,-5.707807541e-01f,8.211025000e-01f,-5.682589412e-01f,8.228498101e-01f,
  -5.657318234e-01f,8.245893121e-01f,-5.631993413e-01f,8.263210654e-01f,-5.606615543e-01f,8.280450702e-01f,-5.581185222e-01f,8.297612071e-01f,
  -5.555702448e-01f,8.314695954e-01f,-5.530167222e-01f,8.331701756e-01f,-5.504579544e-01f,8.348628879e-01f,-5.478940606e-01f,8.365477324e-01f,
  -5.453249812e-01f,8.382247090e-01f,-5.427507758e-01f,8.398938179e-01f,-5.401714444e-01f,8.415549994e-01f,-5.375870466e-01f,8.432082534e-01f,
  -5.349976420e-01f,8.448535800e-01f,-5.324031115e-01f,8.464909196e-01f,-5.298036337e-01f,8.481203318e-01f,-5.271991491e-01f,8.497417569e-01f,
  -5.245896578e-01f,8.513551950e-01f,-5.219752789e-01f,8.529605865e-01f,-5.193560123e-01f,8.545579910e-01f,-5.167317986e-01f,8.561473489e-01f,
  -5.141027570e-01f,8.577286005e-01f,-5.114688277e-01f,8.593018055e-01f,-5.088301301e-01f,8.608669639e-01f,-5.061866641e-01f,8.624239564e-01f,
  -5.035383701e-01f,8.639728427e-01f,-5.008853674e-01f,8.655136228e-01f,-4.982276559e-01f,8.670462370e-01f,-4.955652654e-01f,8.685706854e-01f,
  -4.928981960e-01f,8.700869679e-01f,-4.902264774e-01f,8.715950847e-01f,-4.875501692e-01f,8.730949759e-01f,-4.848692417e-01f,8.745866418e-01f,
  -4.821837842e-01f,8.760700822e-01f,-4.794937670e-01f,8.775452971e-01f,-4.767992198e-01f,8.790122271e-01f,-4.741002023e-01f,8.804708719e-01f,
  -4.713967443e-01f,8.819212914e-01f,-4.686888158e-01f,8.833633661e-01f,-4.659765065e-01f,8.847970963e-01f,-4.632597864e-01f,8.862225413e-01f,
  -4.605387151e-01f,8.876396418e-01f,-4.578132927e-01f,8.890483379e-01f,-4.550835788e-01f,8.904487491e-01f,-4.523495734e-01f,8.918406963e-01f,
  -4.496113360e-01f,8.932242990e-01f,-4.468688369e-01f,8.945994973e-01f,-4.441221356e-01f,8.959662318e-01f,-4.413712621e-01f,8.973245621e-01f,
  -4.386162460e-01f,8.986744881e-01f,-4.358570874e-01f,9.000158906e-01f,-4.330938160e-01f,9.013488293e-01f,-4.303264916e-01f,9.026733041e-01f,
  -4.275550842e-01f,9.039893150e-01f,-4.247796834e-01f,9.052967429e-01f,-4.220002592e-01f,9.065957069e-01f,-4.192169011e-01f,9.078860879e-01f,
  -4.164295495e-01f,9.091680050e-01f,-4.136383235e-01f,9.104412794e-01f,-4.108431637e-01f,9.117060304e-01f,-4.080441594e-01f,9.129621983e-01f,
  -4.052413106e-01f,9.142097831e-01f,-4.024346471e-01f,9.154487252e-01f,-3.996241987e-01f,9.166790843e-01f,-3.968099952e-01f,9.179008007e-01f,
  -3.939920366e-01f,9.191138744e-01f,-3.911703825e-01f,9.203183055e-01f,-3.883450329e-01f,9.215140343e-01f,-3.855160475e-01f,9.227011204e-01f,
  -3.826834261e-01f,9.238795042e-01f,-3.798471987e-01f,9.250492454e-01f,-3.770074248e-01f,9.262102246e-01f,-3.741640747e-01f,9.273625016e-01f,
  -3.713172078e-01f,9.285060763e-01f,-3.684668243e-01f,9.296408892e-01f,-3.656129837e-01f,9.307669401e-01f,-3.627557158e-01f,9.318842888e-01f,
  -3.598950505e-01f,9.329928160e-01f,-3.570309579e-01f,9.340925217e-01f,-3.541635275e-01f,9.351835251e-01f,-3.512927592e-01f,9.362656474e-01f,
  -3.484186828e-01f,9.373390079e-01f,-3.455413282e-01f,9.384035468e-01f,-3.426607251e-01f,9.394592047e-01f,-3.397768736e-01f,9.405060410e-01f,
  -3.368898630e-01f,9.415440559e-01f,-3.339996636e-01f,9.425731897e-01f,-3.311063051e-01f,9.435934424e-01f,-3.282098472e-01f,9.446048141e-01f,
  -3.253102899e-01f,9.456073046e-01f,-3.224076927e-01f,9.466009140e-01f,-3.195020258e-01f,9.475855827e-01f,-3.165933788e-01f,9.485613704e-01f,
  -3.136817515e-01f,9.495281577e-01f,-3.107671440e-01f,9.504860640e-01f,-3.078496456e-01f,9.514350295e-01f,-3.049292266e-01f,9.523749948e-01f,
  -3.020059466e-01f,9.533060193e-01f,-2.990798354e-01f,9.542281032e-01f,-2.961508930e-01f,9.551411867e-01f,-2.932191491e-01f,9.560452700e-01f,
  -2.902846634e-01f,9.569403529e-01f,-2.873474658e-01f,9.578264356e-01f,-2.844075263e-01f,9.587034583e-01f,-2.814649343e-01f,9.595715404e-01f,
  -2.785196900e-01f,9.604305029e-01f,-2.755718231e-01f,9.612804651e-01f,-2.726213634e-01f,9.621214271e-01f,-2.696683109e-01f,9.629532695e-01f,
  -2.667127550e-01f,9.637760520e-01f,-2.637546659e-01f,9.645897746e-01f,-2.607941031e-01f,9.653944373e-01f,-2.578310966e-01f,9.661899805e-01f,
  -2.548656464e-01f,9.669764638e-01f,-2.518978119e-01f,9.677538276e-01f,-2.489276081e-01f,9.685220718e-01f,-2.459550500e-01f,9.692812562e-01f,
  -2.429801822e-01f,9.700312614e-01f,-2.400030196e-01f,9.707721472e-01f,-2.370236069e-01f,9.715039134e-01f,-2.340419590e-01f,9.722265005e-01f,
  -2.310581058e-01f,9.729399681e-01f,-2.280720770e-01f,9.736442566e-01f,-2.250839174e-01f,9.743393660e-01f,-2.220936269e-01f,9.750253558e-01f,
  -2.191012353e-01f,9.757021070e-01f,-2.161068022e-01f,9.763697386e-01f,-2.131103128e-01f,9.770281315e-01f,-2.101118416e-01f,9.776773453e-01f,
  -2.071113735e-01f,9.783173800e-01f,-2.041089684e-01f,9.789481759e-01f,-2.011046410e-01f,9.795697927e-01f,-1.980984062e-01f,9.801821113e-01f,
  -1.950903237e-01f,9.807852507e-01f,-1.920803934e-01f,9.813792109e-01f,-1.890686601e-01f,9.819638729e-01f,-1.860551536e-01f,9.825392962e-01f,
  -1.830398887e-01f,9.831054807e-01f,-1.800228953e-01f,9.836624265e-01f,-1.770042181e-01f,9.842100739e-01f,-1.739838719e-01f,9.847484827e-01f,
  -1.709618866e-01f,9.852776527e-01f,-1.679382920e-01f,9.857975245e-01f,-1.649131179e-01f,9.863080978e-01f,-1.618863940e-01f,9.868093729e-01f,
  -1.588581502e-01f,9.873014092e-01f,-1.558284014e-01f,9.877841473e-01f,-1.527971923e-01f,9.882575870e-01f,-1.497645378e-01f,9.887216687e-01f,
  -1.467304677e-01f,9.891765118e-01f,-1.436950266e-01f,9.896219969e-01f,-1.406582445e-01f,9.900581837e-01f,-1.376201212e-01f,9.904850721e-01f,
  -1.345807016e-01f,9.909026623e-01f,-1.315400302e-01f,9.913108349e-01f,-1.284981072e-01f,9.917097688e-01f,-1.254549772e-01f,9.920992851e-01f,
  -1.224106774e-01f,9.924795628e-01f,-1.193652153e-01f,9.928504229e-01f,-1.163186282e-01f,9.932119250e-01f,-1.132709533e-01f,9.935641289e-01f,
  -1.102222055e-01f,9.939069748e-01f,-1.071724221e-01f,9.942404628e-01f,-1.041216329e-01f,9.945645928e-01f,-1.010698602e-01f,9.948793054e-01f,
  -9.801714122e-02f,9.951847196e-01f,-9.496349841e-02f,9.954807758e-01f,-9.190895408e-02f,9.957674146e-01f,-8.885355294e-02f,9.960446954e-01f,
  -8.579730988e-02f,9.963126183e-01f,-8.274026215e-02f,9.965711236e-01f,-7.968243957e-02f,9.968202710e-01f,-7.662386447e-02f,9.970600605e-01f,
  -7.356456667e-02f,9.972904325e-01f,-7.050457597e-02f,9.975114465e-01f,-6.744392216e-02f,9.977230430e-01f,-6.438262761e-02f,9.979252815e-01f,
  -6.132073700e-02f,9.981181026e-01f,-5.825826526e-02f,9.983015656e-01f,-5.519524589e-02f,9.984755516e-01f,-5.213170499e-02f,9.986402392e-01f,
  -4.906767607e-02f,9.987954497e-01f,-4.600318149e-02f,9.989413023e-01f,-4.293825850e-02f,9.990777373e-01f,-3.987292573e-02f,9.992047548e-01f,
  -3.680722415e-02f,9.993223548e-01f,-3.374117240e-02f,9.994305968e-01f,-3.067480400e-02f,9.995294213e-01f,-2.760814503e-02f,9.996188283e-01f,
  -2.454122901e-02f,9.996988177e-01f,-2.147408016e-02f,9.997693896e-01f,-1.840673015e-02f,9.998306036e-01f,-1.533920597e-02f,9.998823404e-01f,
  -1.227153838e-02f,9.999247193e-01f,-9.203754365e-03f,9.999576211e-01f,-6.135884672e-03f,9.999811649e-01f,-3.067956772e-03f,9.999952912e-01f,
  -1.836970147e-16f,1.000000000e+00f,3.067956772e-03f,9.999952912e-01f,6.135884672e-03f,9.999811649e-01f,9.203754365e-03f,9.999576211e-01f,
  1.227153838e-02f,9.999247193e-01f,1.533920597e-02f,9.998823404e-01f,1.840673015e-02f,9.998306036e-01f,2.147408016e-02f,9.997693896e-01f,
  2.454122901e-02f,9.996988177e-01f,2.760814503e-02f,9.996188283e-01f,3.067480400e-02f,9.995294213e-01f,3.374117240e-02f,9.994305968e-01f,
  3.680722415e-02f,9.993223548e-01f,3.987292573e-02f,9.992047548e-01f,4.293825850e-02f,9.990777373e-01f,4.600318149e-02f,9.989413023e-01f,
  4.906767607e-02f,9.987954497e-01f,5.213170499e-02f,9.986402392e-01f,5.519524589e-02f,9.984755516e-01f,5.825826526e-02f,9.983015656e-01f,
  6.132073700e-02f,9.981181026e-01f,6.438262761e-02f,9.979252815e-01f,6.744392216e-02f,9.977230430e-01f,7.050457597e-02f,9.975114465e-01f,
  7.356456667e-02f,9.972904325e-01f,7.662386447e-02f,9.970600605e-01f,7.968243957e-02f,9.968202710e-01f,8.274026215e-02f,9.965711236e-01f,
  8.579730988e-02f,9.963126183e-01f,8.885355294e-02f,9.960446954e-01f,9.190895408e-02f,9.957674146e-01f,9.496349841e-02f,9.954807758e-01f,
  9.801714122e-02f,9.951847196e-01f,1.010698602e-01f,9.948793054e-01f,1.041216329e-01f,9.945645928e-01f,1.071724221e-01f,9.942404628e-01f,
  1.102222055e-01f,9.939069748e-01f,1.132709533e-01f,9.935641289e-01f,1.163186282e-01f,9.932119250e-01f,1.193652153e-01f,9.928504229e-01f,
  1.224106774e-01f,9.924795628e-01f,1.254549772e-01f,9.920992851e-01f,1.284981072e-01f,9.917097688e-01f,1.315400302e-01f,9.913108349e-01f,
  1.345807016e-01f,9.909026623e-01f,1.376201212e-01f,9.904850721e-01f,1.406582445e-01f,9.900581837e-01f,1.436950266e-01f,9.896219969e-01f,
  1.467304677e-01f,9.891765118e-01f,1.497645378e-01f,9.887216687e-01f,1.527971923e-01f,9.882575870e-01f,1.558284014e-01f,9.877841473e-01f,
  1.588581502e-01f,9.873014092e-01f,1.618863940e-01f,9.868093729e-01f,1.649131179e-01f,9.863080978e-01f,1.679382920e-01f,9.857975245e-01f,
  1.709618866e-01f,9.852776527e-01f,1.739838719e-01f,9.847484827e-01f,1.770042181e-01f,9.842100739e-01f,1.800228953e-01f,9.836624265e-01f,
  1.830398887e-01f,9.831054807e-01f,1.860551536e-01f,9.825392962e-01f,1.890686601e-01f,9.819638729e-01f,1.920803934e-01f,9.813792109e-01f,
  1.950903237e-01f,9.807852507e-01f,1.980984062e-01f,9.801821113e-01f,2.011046410e-01f,9.795697927e-01f,2.041089684e-01f,9.789481759e-01f,
  2.071113735e-01f,9.783173800e-01f,2.101118416e-01f,9.776773453e-01f,2.131103128e-01f,9.770281315e-01f,2.161068022e-01f,9.763697386e-01f,
  2.191012353e-01f,9.757021070e-01f,2.220936269e-01f,9.750253558e-01f,2.250839174e-01f,9.743393660e-01f,2.280720770e-01f,9.736442566e-01f,
  2.310581058e-01f,9.729399681e-01f,2.340419590e-01f,9.722265005e-01f,2.370236069e-01f,9.715039134e-01f,2.400030196e-01f,9.707721472e-01f,
  2.429801822e-01f,9.700312614e-01f,2.459550500e-01f,9.692812562e-01f,2.489276081e-01f,9.685220718e-01f,2.518978119e-01f,9.677538276e-01f,
  2.548656464e-01f,9.669764638e-01f,2.578310966e-01f,9.661899805e-01f,2.607941031e-01f,9.653944373e-01f,2.637546659e-01f,9.645897746e-01f,
  2.667127550e-01f,9.637760520e-01f,2.696683109e-01f,9.629532695e-01f,2.726213634e-01f,9.621214271e-01f,2.755718231e-01f,9.612804651e-01f,
  2.785196900e-01f,9.604305029e-01f,2.814649343e-01f,9.595715404e-01f,2.844075263e-01f,9.587034583e-01f,2.873474658e-01f,9.578264356e-01f,
  2.902846634e-01f,9.569403529e-01f,2.932191491e-01f,9.560452700e-01f,2.961508930e-01f,9.551411867e-01f,2.990798354e-01f,9.542281032e-01f,
  3.020059466e-01f,9.533060193e-01f,3.049292266e-01f,9.523749948e-01f,3.078496456e-01f,9.514350295e-01f,3.107671440e-01f,9.504860640e-01f,
  3.136817515e-01f,9.495281577e-01f,3.165933788e-01f,9.485613704e-01f,3.195020258e-01f,9.475855827e-01f,3.224076927e-01f,9.466009140e-01f,
  3.253102899e-01f,9.456073046e-01f,3.282098472e-01f,9.446048141e-01f,3.311063051e-01f,9.435934424e-01f,3.339996636e-01f,9.425731897e-01f,
  3.368898630e-01f,9.415440559e-01f,3.397768736e-01f,9.405060410e-01f,3.426607251e-01f,9.394592047e-01f,3.455413282e-01f,9.384035468e-01f,
  3.484186828e-01f,9.373390079e-01f,3.512927592e-01f,9.362656474e-01f,3.541635275e-01f,9.351835251e-01f,3.570309579e-01f,9.340925217e-01f,
  3.598950505e-01f,9.329928160e-01f,3.627557158e-01f,9.318842888e-01f,3.656129837e-01f,9.307669401e-01f,3.684668243e-01f,9.296408892e-01f,
  3.713172078e-01f,9.285060763e-01f,3.741640747e-01f,9.273625016e-01f,3.770074248e-01f,9.262102246e-01f,3.798471987e-01f,9.250492454e-01f,
  3.826834261e-01f,9.238795042e-01f,3.855160475e-01f,9.227011204e-01f,3.883450329e-01f,9.215140343e-01f,3.911703825e-01f,9.203183055e-01f,
  3.939920366e-01f,9.191138744e-01f,3.968099952e-01f,9.179008007e-01f,3.996241987e-01f,9.166790843e-01f,4.024346471e-01f,9.154487252e-01f,
  4.052413106e-01f,9.142097831e-01f,4.080441594e-01f,9.129621983e-01f,4.108431637e-01f,9.117060304e-01f,4.136383235e-01f,9.104412794e-01f,
  4.164295495e-01f,9.091680050e-01f,4.192169011e-01f,9.078860879e-01f,4.220002592e-01f,9.065957069e-01f,4.247796834e-01f,9.052967429e-01f,
  4.275550842e-01f,9.039893150e-01f,4.303264916e-01f,9.026733041e-01f,4.330938160e-01f,9.013488293e-01f,4.358570874e-01f,9.000158906e-01f,
  4.386162460e-01f,8.986744881e-01f,4.413712621e-01f,8.973245621e-01f,4.441221356e-01f,8.959662318e-01f,4.468688369e-01f,8.945994973e-01f,
  4.496113360e-01f,8.932242990e-01f,4.523495734e-01f,8.918406963e-01f,4.550835788e-01f,8.904487491e-01f,4.578132927e-01f,8.890483379e-01f,
  4.605387151e-01f,8.876396418e-01f,4.632597864e-01f,8.862225413e-01f,4.659765065e-01f,8.847970963e-01f,4.686888158e-01f,8.833633661e-01f,
  4.713967443e-01f,8.819212914e-01f,4.741002023e-01f,8.804708719e-01f,4.767992198e-01f,8.790122271e-01f,4.794937670e-01f,8.775452971e-01f,
  4.821837842e-01f,8.760700822e-01f,4.848692417e-01f,8.745866418e-01f,4.875501692e-01f,8.730949759e-01f,4.902264774e-01f,8.715950847e-01f,
  4.928981960e-01f,8.700869679e-01f,4.955652654e-01f,8.685706854e-01f,4.982276559e-01f,8.670462370e-01f,5.008853674e-01f,8.655136228e-01f,
  5.035383701e-01f,8.639728427e-01f,5.061866641e-01f,8.624239564e-01f,5.088301301e-01f,8.608669639e-01f,5.114688277e-01f,8.593018055e-01f,
  5.141027570e-01f,8.577286005e-01f,5.167317986e-01f,8.561473489e-01f,5.193560123e-01f,8.545579910e-01f,5.219752789e-01f,8.529605865e-01f,
  5.245896578e-01f,8.513551950e-01f,5.271991491e-01f,8.497417569e-01f,5.298036337e-01f,8.481203318e-01f,5.324031115e-01f,8.464909196e-01f,
  5.349976420e-01f,8.448535800e-01f,5.375870466e-01f,8.432082534e-01f,5.401714444e-01f,8.415549994e-01f,5.427507758e-01f,8.398938179e-01f,
  5.453249812e-01f,8.382247090e-01f,5.478940606e-01f,8.365477324e-01f,5.504579544e-01f,8.348628879e-01f,5.530167222e-01f,8.331701756e-01f,
  5.555702448e-01f,8.314695954e-01f,5.581185222e-01f,8.297612071e-01f,5.606615543e-01f,8.280450702e-01f,5.631993413e-01f,8.263210654e-01f,
  5.657318234e-01f,8.245893121e-01f,5.682589412e-01f,8.228498101e-01f,5.707807541e-01f,8.211025000e-01f,5.732971430e-01f,8.193475008e-01f,
  5.758081675e-01f,8.175848126e-01f,5.783137679e-01f,8.158144355e-01f,5.808139443e-01f,8.140363097e-01f,5.833086371e-01f,8.122506142e-01f,
  5.857978463e-01f,8.104571700e-01f,5.882815719e-01f,8.086561561e-01f,5.907596946e-01f,8.068475723e-01f,5.932322741e-01f,8.050313592e-01f,
  5.956993103e-01f,8.032075167e-01f,5.981606841e-01f,8.013761640e-01f,6.006164551e-01f,7.995372415e-01f,6.030666232e-01f,7.976908684e-01f,
  6.055110693e-01f,7.958369255e-01f,6.079497933e-01f,7.939754725e-01f,6.103827953e-01f,7.921065688e-01f,6.128100753e-01f,7.902302146e-01f,
  6.152315736e-01f,7.883464098e-01f,6.176472902e-01f,7.864552140e-01f,6.200572252e-01f,7.845565677e-01f,6.224612594e-01f,7.826505899e-01f,
  6.248595119e-01f,7.807372212e-01f,6.272518039e-01f,7.788165212e-01f,6.296382546e-01f,7.768884897e-01f,6.320187449e-01f,7.749531269e-01f,
  6.343932748e-01f,7.730104327e-01f,6.367618442e-01f,7.710605264e-01f,6.391244531e-01f,7.691033483e-01f,6.414810419e-01f,7.671388984e-01f,
  6.438315511e-01f,7.651672363e-01f,6.461760402e-01f,7.631884217e-01f,6.485143900e-01f,7.612023950e-01f,6.508466601e-01f,7.592092156e-01f,
  6.531728506e-01f,7.572088242e-01f,6.554928422e-01f,7.552013993e-01f,6.578066945e-01f,7.531868219e-01f,6.601143479e-01f,7.511651516e-01f,
  6.624158025e-01f,7.491363883e-01f,6.647109985e-01f,7.471005917e-01f,6.669999361e-01f,7.450577617e-01f,6.692826152e-01f,7.430079579e-01f,
  6.715589762e-01f,7.409511209e-01f,6.738290191e-01f,7.388873100e-01f,6.760926843e-01f,7.368165851e-01f,6.783500314e-01f,7.347388864e-01f,
  6.806010008e-01f,7.326542735e-01f,6.828455329e-01f,7.305627465e-01f,6.850836873e-01f,7.284643650e-01f,6.873153448e-01f,7.263591290e-01f,
  6.895405650e-01f,7.242470980e-01f,6.917592287e-01f,7.221282125e-01f,6.939714551e-01f,7.200025320e-01f,6.961771250e-01f,7.178700566e-01f,
  6.983762383e-01f,7.157308459e-01f,7.005687952e-01f,7.135848403e-01f,7.027547359e-01f,7.114322186e-01f,7.049340606e-01f,7.092728019e-01f,
  7.071067691e-01f,7.071067691e-01f,7.092728019e-01f,7.049340606e-01f,7.114322186e-01f,7.027547359e-01f,7.135848403e-01f,7.005687952e-01f,
  7.157308459e-01f,6.983762383e-01f,7.178700566e-01f,6.961771250e-01f,7.200025320e-01f,6.939714551e-01f,7.221282125e-01f,6.917592287e-01f,
  7.242470980e-01f,6.895405650e-01f,7.263591290e-01f,6.873153448e-01f,7.284643650e-01f,6.850836873e-01f,7.305627465e-01f,6.828455329e-01f,
  7.326542735e-01f,6.806010008e-01f,7.347388864e-01f,6.783500314e-01f,7.368165851e-01f,6.760926843e-01f,7.388873100e-01f,6.738290191e-01f,
  7.409511209e-01f,6.715589762e-01f,7.430079579e-01f,6.692826152e-01f,7.450577617e-01f,6.669999361e-01f,7.471005917e-01f,6.647109985e-01f,
  7.491363883e-01f,6.624158025e-01f,7.511651516e-01f,6.601143479e-01f,7.531868219e-01f,6.578066945e-01f,7.552013993e-01f,6.554928422e-01f,
  7.572088242e-01f,6.531728506e-01f,7.592092156e-01f,6.508466601e-01f,7.612023950e-01f,6.485143900e-01f,7.631884217e-01f,6.461760402e-01f,
  7.651672363e-01f,6.438315511e-01f,7.671388984e-01f,6.414810419e-01f,7.691033483e-01f,6.391244531e-01f,7.710605264e-01f,6.367618442e-01f,
  7.730104327e-01f,6.343932748e-01f,7.749531269e-01f,6.320187449e-01f,7.768884897e-01f,6.296382546e-01f,7.788165212e-01f,6.272518039e-01f,
  7.807372212e-01f,6.248595119e-01f,7.826505899e-01f,6.224612594e-01f,7.845565677e-01f,6.200572252e-01f,7.864552140e-01f,6.176472902e-01f,
  7.883464098e-01f,6.152315736e-01f,7.902302146e-01f,6.128100753e-01f,7.921065688e-01f,6.103827953e-01f,7.939754725e-01f,6.079497933e-01f,
  7.958369255e-01f,6.055110693e-01f,7.976908684e-01f,6.030666232e-01f,7.995372415e-01f,6.006164551e-01f,8.013761640e-01f,5.981606841e-01f,
  8.032075167e-01f,5.956993103e-01f,8.050313592e-01f,5.932322741e-01f,8.068475723e-01f,5.907596946e-01f,8.086561561e-01f,5.882815719e-01f,
  8.104571700e-01f,5.857978463e-01f,8.122506142e-01f,5.833086371e-01f,8.140363097e-01f,5.808139443e-01f,8.158144355e-01f,5.783137679e-01f,
  8.175848126e-01f,5.758081675e-01f,8.193475008e-01f,5.732971430e-01f,8.211025000e-01f,5.707807541e-01f,8.228498101e-01f,5.682589412e-01f,
  8.245893121e-01f,5.657318234e-01f,8.263210654e-01f,5.631993413e-01f,8.280450702e-01f,5.606615543e-01f,8.297612071e-01f,5.581185222e-01f,
  8.314695954e-01f,5.555702448e-01f,8.331701756e-01f,5.530167222e-01f,8.348628879e-01f,5.504579544e-01f,8.365477324e-01f,5.478940606e-01f,
  8.382247090e-01f,5.453249812e-01f,8.398938179e-01f,5.427507758e-01f,8.415549994e-01f,5.401714444e-01f,8.432082534e-01f,5.375870466e-01f,
  8.448535800e-01f,5.349976420e-01f,8.464909196e-01f,5.324031115e-01f,8.481203318e-01f,5.298036337e-01f,8.497417569e-01f,5.271991491e-01f,
  8.513551950e-01f,5.245896578e-01f,8.529605865e-01f,5.219752789e-01f,8.545579910e-01f,5.193560123e-01f,8.561473489e-01f,5.167317986e-01f,
  8.577286005e-01f,5.141027570e-01f,8.593018055e-01f,5.114688277e-01f,8.608669639e-01f,5.088301301e-01f,8.624239564e-01f,5.061866641e-01f,
  8.639728427e-01f,5.035383701e-01f,8.655136228e-01f,5.008853674e-01f,8.670462370e-01f,4.982276559e-01f,8.685706854e-01f,4.955652654e-01f,
  8.700869679e-01f,4.928981960e-01f,8.715950847e-01f,4.902264774e-01f,8.730949759e-01f,4.875501692e-01f,8.745866418e-01f,4.848692417e-01f,
  8.760700822e-01f,4.821837842e-01f,8.775452971e-01f,4.794937670e-01f,8.790122271e-01f,4.767992198e-01f,8.804708719e-01f,4.741002023e-01f,
  8.819212914e-01f,4.713967443e-01f,8.833633661e-01f,4.686888158e-01f,8.847970963e-01f,4.659765065e-01f,8.862225413e-01f,4.632597864e-01f,
  8.876396418e-01f,4.605387151e-01f,8.890483379e-01f,4.578132927e-01f,8.904487491e-01f,4.550835788e-01f,8.918406963e-01f,4.523495734e-01f,
  8.932242990e-01f,4.496113360e-01f,8.945994973e-01f,4.468688369e-01f,8.959662318e-01f,4.441221356e-01f,8.973245621e-01f,4.413712621e-01f,
  8.986744881e-01f,4.386162460e-01f,9.000158906e-01f,4.358570874e-01f,9.013488293e-01f,4.330938160e-01f,9.026733041e-01f,4.303264916e-01f,
  9.039893150e-01f,4.275550842e-01f,9.052967429e-01f,4.247796834e-01f,9.065957069e-01f,4.220002592e-01f,9.078860879e-01f,4.192169011e-01f,
  9.091680050e-01f,4.164295495e-01f,9.104412794e-01f,4.136383235e-01f,9.117060304e-01f,4.108431637e-01f,9.129621983e-01f,4.080441594e-01f,
  9.142097831e-01f,4.052413106e-01f,9.154487252e-01f,4.024346471e-01f,9.166790843e-01f,3.996241987e-01f,9.179008007e-01f,3.968099952e-01f,
  9.191138744e-01f,3.939920366e-01f,9.203183055e-01f,3.911703825e-01f,9.215140343e-01f,3.883450329e-01f,9.227011204e-01f,3.855160475e-01f,
  9.238795042e-01f,3.826834261e-01f,9.250492454e-01f,3.798471987e-01f,9.262102246e-01f,3.770074248e-01f,9.273625016e-01f,3.741640747e-01f,
  9.285060763e-01f,3.713172078e-01f,9.296408892e-01f,3.684668243e-01f,9.307669401e-01f,3.656129837e-01f,9.318842888e-01f,3.627557158e-01f,
  9.329928160e-01f,3.598950505e-01f,9.340925217e-01f,3.570309579e-01f,9.351835251e-01f,3.541635275e-01f,9.362656474e-01f,3.512927592e-01f,
  9.373390079e-01f,3.484186828e-01f,9.384035468e-01f,3.455413282e-01f,9.394592047e-01f,3.426607251e-01f,9.405060410e-01f,3.397768736e-01f,
  9.415440559e-01f,3.368898630e-01f,9.425731897e-01f,3.339996636e-01f,9.435934424e-01f,3.311063051e-01f,9.446048141e-01f,3.282098472e-01f,
  9.456073046e-01f,3.253102899e-01f,9.466009140e-01f,3.224076927e-01f,9.475855827e-01f,3.195020258e-01f,9.485613704e-01f,3.165933788e-01f,
  9.495281577e-01f,3.136817515e-01f,9.504860640e-01f,3.107671440e-01f,9.514350295e-01f,3.078496456e-01f,9.523749948e-01f,3.049292266e-01f,
  9.533060193e-01f,3.020059466e-01f,9.542281032e-01f,2.990798354e-01f,9.551411867e-01f,2.961508930e-01f,9.560452700e-01f,2.932191491e-01f,
  9.569403529e-01f,2.902846634e-01f,9.578264356e-01f,2.873474658e-01f,9.587034583e-01f,2.844075263e-01f,9.595715404e-01f,2.814649343e-01f,
  9.604305029e-01f,2.785196900e-01f,9.612804651e-01f,2.755718231e-01f,9.621214271e-01f,2.726213634e-01f,9.629532695e-01f,2.696683109e-01f,
  9.637760520e-01f,2.667127550e-01f,9.645897746e-01f,2.637546659e-01f,9.653944373e-01f,2.607941031e-01f,9.661899805e-01f,2.578310966e-01f,
  9.669764638e-01f,2.548656464e-01f,9.677538276e-01f,2.518978119e-01f,9.685220718e-01f,2.489276081e-01f,9.692812562e-01f,2.459550500e-01f,
  9.700312614e-01f,2.429801822e-01f,9.707721472e-01f,2.400030196e-01f,9.715039134e-01f,2.370236069e-01f,9.722265005e-01f,2.340419590e-01f,
  9.729399681e-01f,2.310581058e-01f,9.736442566e-01f,2.280720770e-01f,9.743393660e-01f,2.250839174e-01f,9.750253558e-01f,2.220936269e-01f,
  9.757021070e-01f,2.191012353e-01f,9.763697386e-01f,2.161068022e-01f,9.770281315e-01f,2.131103128e-01f,9.776773453e-01f,2.101118416e-01f,
  9.783173800e-01f,2.071113735e-01f,9.789481759e-01f,2.041089684e-01f,9.795697927e-01f,2.011046410e-01f,9.801821113e-01f,1.980984062e-01f,
  9.807852507e-01f,1.950903237e-01f,9.813792109e-01f,1.920803934e-01f,9.819638729e-01f,1.890686601e-01f,9.825392962e-01f,1.860551536e-01f,
  9.831054807e-01f,1.830398887e-01f,9.836624265e-01f,1.800228953e-01f,9.842100739e-01f,1.770042181e-01f,9.847484827e-01f,1.739838719e-01f,
  9.852776527e-01f,1.709618866e-01f,9.857975245e-01f,1.679382920e-01f,9.863080978e-01f,1.649131179e-01f,9.868093729e-01f,1.618863940e-01f,
  9.873014092e-01f,1.588581502e-01f,9.877841473e-01f,1.558284014e-01f,9.882575870e-01f,1.527971923e-01f,9.887216687e-01f,1.497645378e-01f,
  9.891765118e-01f,1.467304677e-01f,9.896219969e-01f,1.436950266e-01f,9.900581837e-01f,1.406582445e-01f,9.904850721e-01f,1.376201212e-01f,
  9.909026623e-01f,1.345807016e-01f,9.913108349e-01f,1.315400302e-01f,9.917097688e-01f,1.284981072e-01f,9.920992851e-01f,1.254549772e-01f,
  9.924795628e-01f,1.224106774e-01f,9.928504229e-01f,1.193652153e-01f,9.932119250e-01f,1.163186282e-01f,9.935641289e-01f,1.132709533e-01f,
  9.939069748e-01f,1.102222055e-01f,9.942404628e-01f,1.071724221e-01f,9.945645928e-01f,1.041216329e-01f,9.948793054e-01f,1.010698602e-01f,
  9.951847196e-01f,9.801714122e-02f,9.954807758e-01f,9.496349841e-02f,9.957674146e-01f,9.190895408e-02f,9.960446954e-01f,8.885355294e-02f,
  9.963126183e-01f,8.579730988e-02f,9.965711236e-01f,8.274026215e-02f,9.968202710e-01f,7.968243957e-02f,9.970600605e-01f,7.662386447e-02f,
  9.972904325e-01f,7.356456667e-02f,9.975114465e-01f,7.050457597e-02f,9.977230430e-01f,6.744392216e-02f,9.979252815e-01f,6.438262761e-02f,
  9.981181026e-01f,6.132073700e-02f,9.983015656e-01f,5.825826526e-02f,9.984755516e-01f,5.519524589e-02f,9.986402392e-01f,5.213170499e-02f,
  9.987954497e-01f,4.906767607e-02f,9.989413023e-01f,4.600318149e-02f,9.990777373e-01f,4.293825850e-02f,9.992047548e-01f,3.987292573e-02f,
  9.993223548e-01f,3.680722415e-02f,9.994305968e-01f,3.374117240e-02f,9.995294213e-01f,3.067480400e-02f,9.996188283e-01f,2.760814503e-02f,
  9.996988177e-01f,2.454122901e-02f,9.997693896e-01f,2.147408016e-02f,9.998306036e-01f,1.840673015e-02f,9.998823404e-01f,1.533920597e-02f,
  9.999247193e-01f,1.227153838e-02f,9.999576211e-01f,9.203754365e-03f,9.999811649e-01f,6.135884672e-03f,9.999952912e-01f,3.067956772e-03f,
};
__device__ __attribute__((aligned(16))) const float TW576[1152] = {
  1.000000000e+00f,0.000000000e+00f,9.999405146e-01f,-1.090809144e-02f,9.997619987e-01f,-2.181488462e-02f,9.994645715e-01f,-3.271908313e-02f,
  9.990482330e-01f,-4.361938685e-02f,9.985129833e-01f,-5.451450124e-02f,9.978589416e-01f,-6.540312618e-02f,9.970861077e-01f,-7.628397644e-02f,
  9.961947203e-01f,-8.715574443e-02f,9.951847196e-01f,-9.801714122e-02f,9.940563440e-01f,-1.088668779e-01f,9.928096533e-01f,-1.197036579e-01f,
  9.914448857e-01f,-1.305261850e-01f,9.899621010e-01f,-1.413331926e-01f,9.883615375e-01f,-1.521233916e-01f,9.866433144e-01f,-1.628954709e-01f,
  9.848077297e-01f,-1.736481786e-01f,9.828549623e-01f,-1.843802184e-01f,9.807852507e-01f,-1.950903237e-01f,9.785988927e-01f,-2.057772130e-01f,
  9.762960076e-01f,-2.164396197e-01f,9.738769531e-01f,-2.270762622e-01f,9.713420868e-01f,-2.376858890e-01f,9.686915874e-01f,-2.482672334e-01f,
  9.659258127e-01f,-2.588190436e-01f,9.630451202e-01f,-2.693400681e-01f,9.600498676e-01f,-2.798290253e-01f,9.569403529e-01f,-2.902846634e-01f,
  9.537169337e-01f,-3.007057905e-01f,9.503800869e-01f,-3.110911548e-01f,9.469301105e-01f,-3.214394748e-01f,9.433674812e-01f,-3.317495286e-01f,
  9.396926165e-01f,-3.420201540e-01f,9.359059334e-01f,-3.522500396e-01f,9.320078492e-01f,-3.624380529e-01f,9.279989004e-01f,-3.725829124e-01f,
  9.238795042e-01f,-3.826834261e-01f,9.196501970e-01f,-3.927384317e-01f,9.153114557e-01f,-4.027466774e-01f,9.108638167e-01f,-4.127070308e-01f,
  9.063078165e-01f,-4.226182699e-01f,9.016439319e-01f,-4.324792027e-01f,8.968727589e-01f,-4.422886968e-01f,8.919948339e-01f,-4.520455599e-01f,
  8.870108128e-01f,-4.617486000e-01f,8.819212914e-01f,-4.713967443e-01f,8.767267466e-01f,-4.809887707e-01f,8.714279532e-01f,-4.905235767e-01f,
  8.660253882e-01f,-5.000000000e-01f,8.605198264e-01f,-5.094169378e-01f,8.549118638e-01f,-5.187732577e-01f,8.492021561e-01f,-5.280678272e-01f,
  8.433914185e-01f,-5.372996330e-01f,8.374803662e-01f,-5.464674234e-01f,8.314695954e-01f,-5.555702448e-01f,8.253599405e-01f,-5.646069050e-01f,
  8.191520572e-01f,-5.735764503e-01f,8.128466606e-01f,-5.824776888e-01f,8.064445853e-01f,-5.913096666e-01f,7.999465466e-01f,-6.000712514e-01f,
  7.933533192e-01f,-6.087614298e-01f,7.866657376e-01f,-6.173791885e-01f,7.798844576e-01f,-6.259234548e-01f,7.730104327e-01f,-6.343932748e-01f,
  7.660444379e-01f,-6.427876353e-01f,7.589873075e-01f,-6.511054635e-01f,7.518398166e-01f,-6.593458056e-01f,7.446028590e-01f,-6.675077081e-01f,
  7.372773290e-01f,-6.755902171e-01f,7.298640609e-01f,-6.835923195e-01f,7.223639488e-01f,-6.915130615e-01f,7.147778869e-01f,-6.993515491e-01f,
  7.071067691e-01f,-7.071067691e-01f,6.993515491e-01f,-7.147778869e-01f,6.915130615e-01f,-7.223639488e-01f,6.835923195e-01f,-7.298640609e-01f,
  6.755902171e-01f,-7.372773290e-01f,6.675077081e-01f,-7.446028590e-01f,6.593458056e-01f,-7.518398166e-01f,6.511054635e-01f,-7.589873075e-01f,
  6.427876353e-01f,-7.660444379e-01f,6.343932748e-01f,-7.730104327e-01f,6.259234548e-01f,-7.798844576e-01f,6.173791885e-01f,-7.866657376e-01f,
  6.087614298e-01f,-7.933533192e-01f,6.000712514e-01f,-7.999465466e-01f,5.913096666e-01f,-8.064445853e-01f,5.824776888e-01f,-8.128466606e-01f,
  5.735764503e-01f,-8.191520572e-01f,5.646069050e-01f,-8.253599405e-01f,5.555702448e-01f,-8.314695954e-01f,5.464674234e-01f,-8.374803662e-01f,
  5.372996330e-01f,-8.433914185e-01f,5.280678272e-01f,-8.492021561e-01f,5.187732577e-01f,-8.549118638e-01f,5.094169378e-01f,-8.605198264e-01f,
  5.000000000e-01f,-8.660253882e-01f,4.905235767e-01f,-8.714279532e-01f,4.809887707e-01f,-8.767267466e-01f,4.713967443e-01f,-8.819212914e-01f,
  4.617486000e-01f,-8.870108128e-01f,4.520455599e-01f,-8.919948339e-01f,4.422886968e-01f,-8.968727589e-01f,4.324792027e-01f,-9.016439319e-01f,
  4.226182699e-01f,-9.063078165e-01f,4.127070308e-01f,-9.108638167e-01f,4.027466774e-01f,-9.153114557e-01f,3.927384317e-01f,-9.196501970e-01f,
  3.826834261e-01f,-9.238795042e-01f,3.725829124e-01f,-9.279989004e-01f,3.624380529e-01f,-9.320078492e-01f,3.522500396e-01f,-9.359059334e-01f,
  3.420201540e-01f,-9.396926165e-01f,3.317495286e-01f,-9.433674812e-01f,3.214394748e-01f,-9.469301105e-01f,3.110911548e-01f,-9.503800869e-01f,
  3.007057905e-01f,-9.537169337e-01f,2.902846634e-01f,-9.569403529e-01f,2.798290253e-01f,-9.600498676e-01f,2.693400681e-01f,-9.630451202e-01f,
  2.588190436e-01f,-9.659258127e-01f,2.482672334e-01f,-9.686915874e-01f,2.376858890e-01f,-9.713420868e-01f,2.270762622e-01f,-9.738769531e-01f,
  2.164396197e-01f,-9.762960076e-01f,2.057772130e-01f,-9.785988927e-01f,1.950903237e-01f,-9.807852507e-01f,1.843802184e-01f,-9.828549623e-01f,
  1.736481786e-01f,-9.848077297e-01f,1.628954709e-01f,-9.866433144e-01f,1.521233916e-01f,-9.883615375e-01f,1.413331926e-01f,-9.899621010e-01f,
  1.305261850e-01f,-9.914448857e-01f,1.197036579e-01f,-9.928096533e-01f,1.088668779e-01f,-9.940563440e-01f,9.801714122e-02f,-9.951847196e-01f,
  8.715574443e-02f,-9.961947203e-01f,7.628397644e-02f,-9.970861077e-01f,6.540312618e-02f,-9.978589416e-01f,5.451450124e-02f,-9.985129833e-01f,
  4.361938685e-02f,-9.990482330e-01f,3.271908313e-02f,-9.994645715e-01f,2.181488462e-02f,-9.997619987e-01f,1.090809144e-02f,-9.999405146e-01f,
  6.123234263e-17f,-1.000000000e+00f,-1.090809144e-02f,-9.999405146e-01f,-2.181488462e-02f,-9.997619987e-01f,-3.271908313e-02f,-9.994645715e-01f,
  -4.361938685e-02f,-9.990482330e-01f,-5.451450124e-02f,-9.985129833e-01f,-6.540312618e-02f,-9.978589416e-01f,-7.628397644e-02f,-9.970861077e-01f,
  -8.715574443e-02f,-9.961947203e-01f,-9.801714122e-02f,-9.951847196e-01f,-1.088668779e-01f,-9.940563440e-01f,-1.197036579e-01f,-9.928096533e-01f,
  -1.305261850e-01f,-9.914448857e-01f,-1.413331926e-01f,-9.899621010e-01f,-1.521233916e-01f,-9.883615375e-01f,-1.628954709e-01f,-9.866433144e-01f,
  -1.736481786e-01f,-9.848077297e-01f,-1.843802184e-01f,-9.828549623e-01f,-1.950903237e-01f,-9.807852507e-01f,-2.057772130e-01f,-9.785988927e-01f,
  -2.164396197e-01f,-9.762960076e-01f,-2.270762622e-01f,-9.738769531e-01f,-2.376858890e-01f,-9.713420868e-01f,-2.482672334e-01f,-9.686915874e-01f,
  -2.588190436e-01f,-9.659258127e-01f,-2.693400681e-01f,-9.630451202e-01f,-2.798290253e-01f,-9.600498676e-01f,-2.902846634e-01f,-9.569403529e-01f,
  -3.007057905e-01f,-9.537169337e-01f,-3.110911548e-01f,-9.503800869e-01f,-3.214394748e-01f,-9.469301105e-01f,-3.317495286e-01f,-9.433674812e-01f,
  -3.420201540e-01f,-9.396926165e-01f,-3.522500396e-01f,-9.359059334e-01f,-3.624380529e-01f,-9.320078492e-01f,-3.725829124e-01f,-9.279989004e-01f,
  -3.826834261e-01f,-9.238795042e-01f,-3.927384317e-01f,-9.196501970e-01f,-4.027466774e-01f,-9.153114557e-01f,-4.127070308e-01f,-9.108638167e-01f,
  -4.226182699e-01f,-9.063078165e-01f,-4.324792027e-01f,-9.016439319e-01f,-4.422886968e-01f,-8.968727589e-01f,-4.520455599e-01f,-8.919948339e-01f,
  -4.617486000e-01f,-8.870108128e-01f,-4.713967443e-01f,-8.819212914e-01f,-4.809887707e-01f,-8.767267466e-01f,-4.905235767e-01f,-8.714279532e-01f,
  -5.000000000e-01f,-8.660253882e-01f,-5.094169378e-01f,-8.605198264e-01f,-5.187732577e-01f,-8.549118638e-01f,-5.280678272e-01f,-8.492021561e-01f,
  -5.372996330e-01f,-8.433914185e-01f,-5.464674234e-01f,-8.374803662e-01f,-5.555702448e-01f,-8.314695954e-01f,-5.646069050e-01f,-8.253599405e-01f,
  -5.735764503e-01f,-8.191520572e-01f,-5.824776888e-01f,-8.128466606e-01f,-5.913096666e-01f,-8.064445853e-01f,-6.000712514e-01f,-7.999465466e-01f,
  -6.087614298e-01f,-7.933533192e-01f,-6.173791885e-01f,-7.866657376e-01f,-6.259234548e-01f,-7.798844576e-01f,-6.343932748e-01f,-7.730104327e-01f,
  -6.427876353e-01f,-7.660444379e-01f,-6.511054635e-01f,-7.589873075e-01f,-6.593458056e-01f,-7.518398166e-01f,-6.675077081e-01f,-7.446028590e-01f,
  -6.755902171e-01f,-7.372773290e-01f,-6.835923195e-01f,-7.298640609e-01f,-6.915130615e-01f,-7.223639488e-01f,-6.993515491e-01f,-7.147778869e-01f,
  -7.071067691e-01f,-7.071067691e-01f,-7.147778869e-01f,-6.993515491e-01f,-7.223639488e-01f,-6.915130615e-01f,-7.298640609e-01f,-6.835923195e-01f,
  -7.372773290e-01f,-6.755902171e-01f,-7.446028590e-01f,-6.675077081e-01f,-7.518398166e-01f,-6.593458056e-01f,-7.589873075e-01f,-6.511054635e-01f,
  -7.660444379e-01f,-6.427876353e-01f,-7.730104327e-01f,-6.343932748e-01f,-7.798844576e-01f,-6.259234548e-01f,-7.866657376e-01f,-6.173791885e-01f,
  -7.933533192e-01f,-6.087614298e-01f,-7.999465466e-01f,-6.000712514e-01f,-8.064445853e-01f,-5.913096666e-01f,-8.128466606e-01f,-5.824776888e-01f,
  -8.191520572e-01f,-5.735764503e-01f,-8.253599405e-01f,-5.646069050e-01f,-8.314695954e-01f,-5.555702448e-01f,-8.374803662e-01f,-5.464674234e-01f,
  -8.433914185e-01f,-5.372996330e-01f,-8.492021561e-01f,-5.280678272e-01f,-8.549118638e-01f,-5.187732577e-01f,-8.605198264e-01f,-5.094169378e-01f,
  -8.660253882e-01f,-5.000000000e-01f,-8.714279532e-01f,-4.905235767e-01f,-8.767267466e-01f,-4.809887707e-01f,-8.819212914e-01f,-4.713967443e-01f,
  -8.870108128e-01f,-4.617486000e-01f,-8.919948339e-01f,-4.520455599e-01f,-8.968727589e-01f,-4.422886968e-01f,-9.016439319e-01f,-4.324792027e-01f,
  -9.063078165e-01f,-4.226182699e-01f,-9.108638167e-01f,-4.127070308e-01f,-9.153114557e-01f,-4.027466774e-01f,-9.196501970e-01f,-3.927384317e-01f,
  -9.238795042e-01f,-3.826834261e-01f,-9.279989004e-01f,-3.725829124e-01f,-9.320078492e-01f,-3.624380529e-01f,-9.359059334e-01f,-3.522500396e-01f,
  -9.396926165e-01f,-3.420201540e-01f,-9.433674812e-01f,-3.317495286e-01f,-9.469301105e-01f,-3.214394748e-01f,-9.503800869e-01f,-3.110911548e-01f,
  -9.537169337e-01f,-3.007057905e-01f,-9.569403529e-01f,-2.902846634e-01f,-9.600498676e-01f,-2.798290253e-01f,-9.630451202e-01f,-2.693400681e-01f,
  -9.659258127e-01f,-2.588190436e-01f,-9.686915874e-01f,-2.482672334e-01f,-9.713420868e-01f,-2.376858890e-01f,-9.738769531e-01f,-2.270762622e-01f,
  -9.762960076e-01f,-2.164396197e-01f,-9.785988927e-01f,-2.057772130e-01f,-9.807852507e-01f,-1.950903237e-01f,-9.828549623e-01f,-1.843802184e-01f,
  -9.848077297e-01f,-1.736481786e-01f,-9.866433144e-01f,-1.628954709e-01f,-9.883615375e-01f,-1.521233916e-01f,-9.899621010e-01f,-1.413331926e-01f,
  -9.914448857e-01f,-1.305261850e-01f,-9.928096533e-01f,-1.197036579e-01f,-9.940563440e-01f,-1.088668779e-01f,-9.951847196e-01f,-9.801714122e-02f,
  -9.961947203e-01f,-8.715574443e-02f,-9.970861077e-01f,-7.628397644e-02f,-9.978589416e-01f,-6.540312618e-02f,-9.985129833e-01f,-5.451450124e-02f,
  -9.990482330e-01f,-4.361938685e-02f,-9.994645715e-01f,-3.271908313e-02f,-9.997619987e-01f,-2.181488462e-02f,-9.999405146e-01f,-1.090809144e-02f,
  -1.000000000e+00f,-1.224646853e-16f,-9.999405146e-01f,1.090809144e-02f,-9.997619987e-01f,2.181488462e-02f,-9.994645715e-01f,3.271908313e-02f,
  -9.990482330e-01f,4.361938685e-02f,-9.985129833e-01f,5.451450124e-02f,-9.978589416e-01f,6.540312618e-02f,-9.970861077e-01f,7.628397644e-02f,
  -9.961947203e-01f,8.715574443e-02f,-9.951847196e-01f,9.801714122e-02f,-9.940563440e-01f,1.088668779e-01f,-9.928096533e-01f,1.197036579e-01f,
  -9.914448857e-01f,1.305261850e-01f,-9.899621010e-01f,1.413331926e-01f,-9.883615375e-01f,1.521233916e-01f,-9.866433144e-01f,1.628954709e-01f,
  -9.848077297e-01f,1.736481786e-01f,-9.828549623e-01f,1.843802184e-01f,-9.807852507e-01f,1.950903237e-01f,-9.785988927e-01f,2.057772130e-01f,
  -9.762960076e-01f,2.164396197e-01f,-9.738769531e-01f,2.270762622e-01f,-9.713420868e-01f,2.376858890e-01f,-9.686915874e-01f,2.482672334e-01f,
  -9.659258127e-01f,2.588190436e-01f,-9.630451202e-01f,2.693400681e-01f,-9.600498676e-01f,2.798290253e-01f,-9.569403529e-01f,2.902846634e-01f,
  -9.537169337e-01f,3.007057905e-01f,-9.503800869e-01f,3.110911548e-01f,-9.469301105e-01f,3.214394748e-01f,-9.433674812e-01f,3.317495286e-01f,
  -9.396926165e-01f,3.420201540e-01f,-9.359059334e-01f,3.522500396e-01f,-9.320078492e-01f,3.624380529e-01f,-9.279989004e-01f,3.725829124e-01f,
  -9.238795042e-01f,3.826834261e-01f,-9.196501970e-01f,3.927384317e-01f,-9.153114557e-01f,4.027466774e-01f,-9.108638167e-01f,4.127070308e-01f,
  -9.063078165e-01f,4.226182699e-01f,-9.016439319e-01f,4.324792027e-01f,-8.968727589e-01f,4.422886968e-01f,-8.919948339e-01f,4.520455599e-01f,
  -8.870108128e-01f,4.617486000e-01f,-8.819212914e-01f,4.713967443e-01f,-8.767267466e-01f,4.809887707e-01f,-8.714279532e-01f,4.905235767e-01f,
  -8.660253882e-01f,5.000000000e-01f,-8.605198264e-01f,5.094169378e-01f,-8.549118638e-01f,5.187732577e-01f,-8.492021561e-01f,5.280678272e-01f,
  -8.433914185e-01f,5.372996330e-01f,-8.374803662e-01f,5.464674234e-01f,-8.314695954e-01f,5.555702448e-01f,-8.253599405e-01f,5.646069050e-01f,
  -8.191520572e-01f,5.735764503e-01f,-8.128466606e-01f,5.824776888e-01f,-8.064445853e-01f,5.913096666e-01f,-7.999465466e-01f,6.000712514e-01f,
  -7.933533192e-01f,6.087614298e-01f,-7.866657376e-01f,6.173791885e-01f,-7.798844576e-01f,6.259234548e-01f,-7.730104327e-01f,6.343932748e-01f,
  -7.660444379e-01f,6.427876353e-01f,-7.589873075e-01f,6.511054635e-01f,-7.518398166e-01f,6.593458056e-01f,-7.446028590e-01f,6.675077081e-01f,
  -7.372773290e-01f,6.755902171e-01f,-7.298640609e-01f,6.835923195e-01f,-7.223639488e-01f,6.915130615e-01f,-7.147778869e-01f,6.993515491e-01f,
  -7.071067691e-01f,7.071067691e-01f,-6.993515491e-01f,7.147778869e-01f,-6.915130615e-01f,7.223639488e-01f,-6.835923195e-01f,7.298640609e-01f,
  -6.755902171e-01f,7.372773290e-01f,-6.675077081e-01f,7.446028590e-01f,-6.593458056e-01f,7.518398166e-01f,-6.511054635e-01f,7.589873075e-01f,
  -6.427876353e-01f,7.660444379e-01f,-6.343932748e-01f,7.730104327e-01f,-6.259234548e-01f,7.798844576e-01f,-6.173791885e-01f,7.866657376e-01f,
  -6.087614298e-01f,7.933533192e-01f,-6.000712514e-01f,7.999465466e-01f,-5.913096666e-01f,8.064445853e-01f,-5.824776888e-01f,8.128466606e-01f,
  -5.735764503e-01f,8.191520572e-01f,-5.646069050e-01f,8.253599405e-01f,-5.555702448e-01f,8.314695954e-01f,-5.464674234e-01f,8.374803662e-01f,
  -5.372996330e-01f,8.433914185e-01f,-5.280678272e-01f,8.492021561e-01f,-5.187732577e-01f,8.549118638e-01f,-5.094169378e-01f,8.605198264e-01f,
  -5.000000000e-01f,8.660253882e-01f,-4.905235767e-01f,8.714279532e-01f,-4.809887707e-01f,8.767267466e-01f,-4.713967443e-01f,8.819212914e-01f,
  -4.617486000e-01f,8.870108128e-01f,-4.520455599e-01f,8.919948339e-01f,-4.422886968e-01f,8.968727589e-01f,-4.324792027e-01f,9.016439319e-01f,
  -4.226182699e-01f,9.063078165e-01f,-4.127070308e-01f,9.108638167e-01f,-4.027466774e-01f,9.153114557e-01f,-3.927384317e-01f,9.196501970e-01f,
  -3.826834261e-01f,9.238795042e-01f,-3.725829124e-01f,9.279989004e-01f,-3.624380529e-01f,9.320078492e-01f,-3.522500396e-01f,9.359059334e-01f,
  -3.420201540e-01f,9.396926165e-01f,-3.317495286e-01f,9.433674812e-01f,-3.214394748e-01f,9.469301105e-01f,-3.110911548e-01f,9.503800869e-01f,
  -3.007057905e-01f,9.537169337e-01f,-2.902846634e-01f,9.569403529e-01f,-2.798290253e-01f,9.600498676e-01f,-2.693400681e-01f,9.630451202e-01f,
  -2.588190436e-01f,9.659258127e-01f,-2.482672334e-01f,9.686915874e-01f,-2.376858890e-01f,9.713420868e-01f,-2.270762622e-01f,9.738769531e-01f,
  -2.164396197e-01f,9.762960076e-01f,-2.057772130e-01f,9.785988927e-01f,-1.950903237e-01f,9.807852507e-01f,-1.843802184e-01f,9.828549623e-01f,
  -1.736481786e-01f,9.848077297e-01f,-1.628954709e-01f,9.866433144e-01f,-1.521233916e-01f,9.883615375e-01f,-1.413331926e-01f,9.899621010e-01f,
  -1.305261850e-01f,9.914448857e-01f,-1.197036579e-01f,9.928096533e-01f,-1.088668779e-01f,9.940563440e-01f,-9.801714122e-02f,9.951847196e-01f,
  -8.715574443e-02f,9.961947203e-01f,-7.628397644e-02f,9.970861077e-01f,-6.540312618e-02f,9.978589416e-01f,-5.451450124e-02f,9.985129833e-01f,
  -4.361938685e-02f,9.990482330e-01f,-3.271908313e-02f,9.994645715e-01f,-2.181488462e-02f,9.997619987e-01f,-1.090809144e-02f,9.999405146e-01f,
  -1.836970147e-16f,1.000000000e+00f,1.090809144e-02f,9.999405146e-01f,2.181488462e-02f,9.997619987e-01f,3.271908313e-02f,9.994645715e-01f,
  4.361938685e-02f,9.990482330e-01f,5.451450124e-02f,9.985129833e-01f,6.540312618e-02f,9.978589416e-01f,7.628397644e-02f,9.970861077e-01f,
  8.715574443e-02f,9.961947203e-01f,9.801714122e-02f,9.951847196e-01f,1.088668779e-01f,9.940563440e-01f,1.197036579e-01f,9.928096533e-01f,
  1.305261850e-01f,9.914448857e-01f,1.413331926e-01f,9.899621010e-01f,1.521233916e-01f,9.883615375e-01f,1.628954709e-01f,9.866433144e-01f,
  1.736481786e-01f,9.848077297e-01f,1.843802184e-01f,9.828549623e-01f,1.950903237e-01f,9.807852507e-01f,2.057772130e-01f,9.785988927e-01f,
  2.164396197e-01f,9.762960076e-01f,2.270762622e-01f,9.738769531e-01f,2.376858890e-01f,9.713420868e-01f,2.482672334e-01f,9.686915874e-01f,
  2.588190436e-01f,9.659258127e-01f,2.693400681e-01f,9.630451202e-01f,2.798290253e-01f,9.600498676e-01f,2.902846634e-01f,9.569403529e-01f,
  3.007057905e-01f,9.537169337e-01f,3.110911548e-01f,9.503800869e-01f,3.214394748e-01f,9.469301105e-01f,3.317495286e-01f,9.433674812e-01f,
  3.420201540e-01f,9.396926165e-01f,3.522500396e-01f,9.359059334e-01f,3.624380529e-01f,9.320078492e-01f,3.725829124e-01f,9.279989004e-01f,
  3.826834261e-01f,9.238795042e-01f,3.927384317e-01f,9.196501970e-01f,4.027466774e-01f,9.153114557e-01f,4.127070308e-01f,9.108638167e-01f,
  4.226182699e-01f,9.063078165e-01f,4.324792027e-01f,9.016439319e-01f,4.422886968e-01f,8.968727589e-01f,4.520455599e-01f,8.919948339e-01f,
  4.617486000e-01f,8.870108128e-01f,4.713967443e-01f,8.819212914e-01f,4.809887707e-01f,8.767267466e-01f,4.905235767e-01f,8.714279532e-01f,
  5.000000000e-01f,8.660253882e-01f,5.094169378e-01f,8.605198264e-01f,5.187732577e-01f,8.549118638e-01f,5.280678272e-01f,8.492021561e-01f,
  5.372996330e-01f,8.433914185e-01f,5.464674234e-01f,8.374803662e-01f,5.555702448e-01f,8.314695954e-01f,5.646069050e-01f,8.253599405e-01f,
  5.735764503e-01f,8.191520572e-01f,5.824776888e-01f,8.128466606e-01f,5.913096666e-01f,8.064445853e-01f,6.000712514e-01f,7.999465466e-01f,
  6.087614298e-01f,7.933533192e-01f,6.173791885e-01f,7.866657376e-01f,6.259234548e-01f,7.798844576e-01f,6.343932748e-01f,7.730104327e-01f,
  6.427876353e-01f,7.660444379e-01f,6.511054635e-01f,7.589873075e-01f,6.593458056e-01f,7.518398166e-01f,6.675077081e-01f,7.446028590e-01f,
  6.755902171e-01f,7.372773290e-01f,6.835923195e-01f,7.298640609e-01f,6.915130615e-01f,7.223639488e-01f,6.993515491e-01f,7.147778869e-01f,
  7.071067691e-01f,7.071067691e-01f,7.147778869e-01f,6.993515491e-01f,7.223639488e-01f,6.915130615e-01f,7.298640609e-01f,6.835923195e-01f,
  7.372773290e-01f,6.755902171e-01f,7.446028590e-01f,6.675077081e-01f,7.518398166e-01f,6.593458056e-01f,7.589873075e-01f,6.511054635e-01f,
  7.660444379e-01f,6.427876353e-01f,7.730104327e-01f,6.343932748e-01f,7.798844576e-01f,6.259234548e-01f,7.866657376e-01f,6.173791885e-01f,
  7.933533192e-01f,6.087614298e-01f,7.999465466e-01f,6.000712514e-01f,8.064445853e-01f,5.913096666e-01f,8.128466606e-01f,5.824776888e-01f,
  8.191520572e-01f,5.735764503e-01f,8.253599405e-01f,5.646069050e-01f,8.314695954e-01f,5.555702448e-01f,8.374803662e-01f,5.464674234e-01f,
  8.433914185e-01f,5.372996330e-01f,8.492021561e-01f,5.280678272e-01f,8.549118638e-01f,5.187732577e-01f,8.605198264e-01f,5.094169378e-01f,
  8.660253882e-01f,5.000000000e-01f,8.714279532e-01f,4.905235767e-01f,8.767267466e-01f,4.809887707e-01f,8.819212914e-01f,4.713967443e-01f,
  8.870108128e-01f,4.617486000e-01f,8.919948339e-01f,4.520455599e-01f,8.968727589e-01f,4.422886968e-01f,9.016439319e-01f,4.324792027e-01f,
  9.063078165e-01f,4.226182699e-01f,9.108638167e-01f,4.127070308e-01f,9.153114557e-01f,4.027466774e-01f,9.196501970e-01f,3.927384317e-01f,
  9.238795042e-01f,3.826834261e-01f,9.279989004e-01f,3.725829124e-01f,9.320078492e-01f,3.624380529e-01f,9.359059334e-01f,3.522500396e-01f,
  9.396926165e-01f,3.420201540e-01f,9.433674812e-01f,3.317495286e-01f,9.469301105e-01f,3.214394748e-01f,9.503800869e-01f,3.110911548e-01f,
  9.537169337e-01f,3.007057905e-01f,9.569403529e-01f,2.902846634e-01f,9.600498676e-01f,2.798290253e-01f,9.630451202e-01f,2.693400681e-01f,
  9.659258127e-01f,2.588190436e-01f,9.686915874e-01f,2.482672334e-01f,9.713420868e-01f,2.376858890e-01f,9.738769531e-01f,2.270762622e-01f,
  9.762960076e-01f,2.164396197e-01f,9.785988927e-01f,2.057772130e-01f,9.807852507e-01f,1.950903237e-01f,9.828549623e-01f,1.843802184e-01f,
  9.848077297e-01f,1.736481786e-01f,9.866433144e-01f,1.628954709e-01f,9.883615375e-01f,1.521233916e-01f,9.899621010e-01f,1.413331926e-01f,
  9.914448857e-01f,1.305261850e-01f,9.928096533e-01f,1.197036579e-01f,9.940563440e-01f,1.088668779e-01f,9.951847196e-01f,9.801714122e-02f,
  9.961947203e-01f,8.715574443e-02f,9.970861077e-01f,7.628397644e-02f,9.978589416e-01f,6.540312618e-02f,9.985129833e-01f,5.451450124e-02f,
  9.990482330e-01f,4.361938685e-02f,9.994645715e-01f,3.271908313e-02f,9.997619987e-01f,2.181488462e-02f,9.999405146e-01f,1.090809144e-02f,
};

#define TSTAMP(kid, slot)
struct cf { float x, y; };
__device__ __forceinline__ cf operator+(cf a, cf b) { return cf{a.x + b.x, a.y + b.y}; }
__device__ __forceinline__ cf operator-(cf a, cf b) { return cf{a.x - b.x, a.y - b.y}; }
__device__ __forceinline__ cf cmul(cf a, cf b) { return cf{a.x * b.x - a.y * b.y, a.x * b.y + a.y * b.x}; }

__device__ __forceinline__ float bf2f(u16 v) { return __uint_as_float(((uint32_t)v) << 16); }
__device__ __forceinline__ u16 f2bf(float f) {
  uint32_t u = __float_as_uint(f);
  u += 0x7fffu + ((u >> 16) & 1u);
  return (u16)(u >> 16);
}
__device__ __forceinline__ float rbf(float f) { return bf2f(f2bf(f)); }

__device__ __forceinline__ float wave_sum(float v) {
#pragma unroll
  for (int o = 32; o > 0; o >>= 1) v += __shfl_xor(v, o, 64);
  return v;
}

template <int DIR> __device__ __forceinline__ cf tw(const float* tab, int m) {
  float2 w = *reinterpret_cast<const float2*>(tab + 2 * m);
  return DIR > 0 ? cf{w.x, w.y} : cf{w.x, -w.y};
}
template <int DIR> __device__ __forceinline__ cf mul_mi(cf a) { return DIR > 0 ? cf{a.y, -a.x} : cf{-a.y, a.x}; }

template <int DIR> __device__ __forceinline__ void fft4(cf& a0, cf& a1, cf& a2, cf& a3) {
  cf t0 = a0 + a2, t1 = a0 - a2, t2 = a1 + a3, t3 = mul_mi<DIR>(a1 - a3);
  a0 = t0 + t2; a2 = t0 - t2; a1 = t1 + t3; a3 = t1 - t3;
}
template <int DIR> __device__ __forceinline__ void fft8(cf* a) {
  fft4<DIR>(a[0], a[2], a[4], a[6]);
  fft4<DIR>(a[1], a[3], a[5], a[7]);
  const float h = 0.70710678118654752440f;
  cf o1 = DIR > 0 ? cf{(a[3].x + a[3].y) * h, (a[3].y - a[3].x) * h} : cf{(a[3].x - a[3].y) * h, (a[3].y + a[3].x) * h};
  cf o2 = mul_mi<DIR>(a[5]);
  cf o3 = DIR > 0 ? cf{(-a[7].x + a[7].y) * h, (-a[7].y - a[7].x) * h} : cf{(-a[7].x - a[7].y) * h, (-a[7].y + a[7].x) * h};
  cf o0 = a[1];
  cf e0 = a[0], e1 = a[2], e2 = a[4], e3 = a[6];
  a[0] = e0 + o0; a[4] = e0 - o0;
  a[1] = e1 + o1; a[5] = e1 - o1;
  a[2] = e2 + o2; a[6] = e2 - o2;
  a[3] = e3 + o3; a[7] = e3 - o3;
}
template <int DIR> __device__ __forceinline__ void fft3(cf& a0, cf& a1, cf& a2) {
  cf t1 = a1 + a2, t2 = a1 - a2;
  cf m = cf{a0.x - 0.5f * t1.x, a0.y - 0.5f * t1.y};
  const float s = 0.86602540378443864676f;
  cf n = mul_mi<DIR>(cf{t2.x * s, t2.y * s});
  a0 = a0 + t1; a1 = m + n; a2 = m - n;
}
template <int DIR> __device__ __forceinline__ void fft9(cf* a) {
  fft3<DIR>(a[0], a[3], a[6]); fft3<DIR>(a[1], a[4], a[7]); fft3<DIR>(a[2], a[5], a[8]);
  const float c1 = 0.76604444311897803520f, s1 = 0.64278760968653932632f;
  const float c2 = 0.17364817766693034885f, s2 = 0.98480775301220805937f;
  const float c4 = -0.93969262078590838405f, s4 = 0.34202014332566873304f;
  const cf w1 = cf{c1, DIR > 0 ? -s1 : s1}, w2 = cf{c2, DIR > 0 ? -s2 : s2}, w4 = cf{c4, DIR > 0 ? -s4 : s4};
  a[4] = cmul(a[4], w1); a[5] = cmul(a[5], w2); a[7] = cmul(a[7], w2); a[8] = cmul(a[8], w4);
  fft3<DIR>(a[0], a[1], a[2]); fft3<DIR>(a[3], a[4], a[5]); fft3<DIR>(a[6], a[7], a[8]);
  cf t;
  t = a[1]; a[1] = a[3]; a[3] = t;
  t = a[2]; a[2] = a[6]; a[6] = t;
  t = a[5]; a[5] = a[7]; a[7] = t;
}

__device__ __forceinline__ float modval(const float* modp, int idx) {
  return rbf(modp[idx] + modp[3456 + idx] + modp[6912 + idx] + modp[10368 + idx]);
}

__global__ __launch_bounds__(256) void k0_prep(const u16* __restrict__ c, const u16* __restrict__ wmod,
                                               const u16* __restrict__ wr, const u16* __restrict__ wi,
                                               float* __restrict__ modp, uint32_t* __restrict__ WT) {
  __shared__ __attribute__((aligned(16))) u16 raw[2 * 9248];
  const int b = blockIdx.x, t = threadIdx.x;
  if (b < 108) {
    const int cg = b % 27, kq = b / 27;
    const int cc = t & 15, rg = t >> 4;
    const int col = cg * 128 + cc * 8;
    uint4 w[9];
    float sv[9];
#pragma unroll
    for (int i = 0; i < 9; i++) {
      const int r = kq * 144 + rg + 16 * i;
      w[i] = *reinterpret_cast<const uint4*>(wmod + (size_t)r * 3456 + col);
      sv[i] = bf2f(c[r]);
    }
    float a[8];
#pragma unroll
    for (int j = 0; j < 8; j++) a[j] = 0.f;
#pragma unroll
    for (int i = 0; i < 9; i++) {
      const float s = rbf(sv[i] / (1.f + expf(-sv[i])));
      const uint32_t u[4] = {w[i].x, w[i].y, w[i].z, w[i].w};
#pragma unroll
      for (int j = 0; j < 4; j++) {
        a[2 * j] += s * __uint_as_float(u[j] << 16);
        a[2 * j + 1] += s * __uint_as_float(u[j] & 0xffff0000u);
      }
    }
    float* red = reinterpret_cast<float*>(raw);
#pragma unroll
    for (int j = 0; j < 8; j++) red[rg * 128 + cc * 8 + j] = a[j];
    __syncthreads();
    if (t < 128) {
      float s = 0.f;
#pragma unroll
      for (int g = 0; g < 16; g++) s += red[g * 128 + t];
      modp[kq * 3456 + cg * 128 + t] = s;
    }
  } else {
    const int s0 = (b - 108) * 32;
    const uint4* gr = reinterpret_cast<const uint4*>(wr + (size_t)s0 * DF_);
    const uint4* gi = reinterpret_cast<const uint4*>(wi + (size_t)s0 * DF_);
    uint4* lr = reinterpret_cast<uint4*>(raw);
#pragma unroll
    for (int i = 0; i < 5; i++) {
      const int j = t + 256 * i;
      if (j < 1156) { lr[j] = gr[j]; lr[1156 + j] = gi[j]; }
    }
    __syncthreads();
    for (int k = t >> 5; k < DF_; k += 8)
      WT[k * S_ + s0 + (t & 31)] = (uint32_t)raw[(t & 31) * DF_ + k] | ((uint32_t)raw[9248 + (t & 31) * DF_ + k] << 16);
  }
}

__global__ __launch_bounds__(256) void k1_ln_rowfft(const u16* __restrict__ x, const float* __restrict__ modp,
                                                    cf* __restrict__ X1T) {
  __shared__ cf bA[4 * 576];
  __shared__ cf bB[4 * 576];
  const int t = threadIdx.x, w = t >> 6, l = t & 63;
  const int row0 = blockIdx.x * 8;
  TSTAMP(2, 0);
  __shared__ float smod[1152];
  __shared__ __attribute__((aligned(16))) float sTW[1152];
  u16 xr[2][9];
#pragma unroll
  for (int rr = 0; rr < 2; rr++)
#pragma unroll
    for (int i = 0; i < 9; i++) xr[rr][i] = x[(row0 + 2 * w + rr) * D_ + l + 64 * i];
  {
    float mv[5];
#pragma unroll
    for (int j = 0; j < 5; j++) { const int i = t + 256 * j; mv[j] = modval(modp, i < 1152 ? i : 0); }
#pragma unroll
    for (int j = 0; j < 5; j++) { const int i = t + 256 * j; if (i < 1152) smod[i] = i < 576 ? mv[j] : rbf(1.f + mv[j]); }
  }
  for (int i = t; i < 288; i += 256) reinterpret_cast<float4*>(sTW)[i] = reinterpret_cast<const float4*>(TW576)[i];
  __syncthreads();
  float sa[9], t1[9];
#pragma unroll
  for (int i = 0; i < 9; i++) { sa[i] = smod[l + 64 * i]; t1[i] = smod[576 + l + 64 * i]; }
#pragma unroll
  for (int rr = 0; rr < 2; rr++) {
    const int row = row0 + 2 * w + rr;
    float xv[9];
    float s = 0.f;
#pragma unroll
    for (int i = 0; i < 9; i++) { xv[i] = bf2f(xr[rr][i]); s += xv[i]; }
    s = wave_sum(s);
    const float mean = s * (1.f / 576.f);
    float q = 0.f;
#pragma unroll
    for (int i = 0; i < 9; i++) { float d = xv[i] - mean; q += d * d; }
    q = wave_sum(q);
    const float m = rbf(mean), v = rbf(q * (1.f / 576.f));
    const float sq = rbf(sqrtf(rbf(v + 1e-5f)));
#pragma unroll
    for (int i = 0; i < 9; i++) {
      float d = rbf(xv[i] - m);
      float z = rbf(d / sq);
      float h = rbf(rbf(z * t1[i]) + sa[i]);
      if (rr == 0) bA[w * 576 + l + 64 * i].x = h; else bA[w * 576 + l + 64 * i].y = h;
    }
  }
  __syncthreads();
  TSTAMP(2, 1);
  {
    const int seq = t >> 6, j = t & 63;
    cf v[9];
#pragma unroll
    for (int r = 0; r < 9; r++) v[r] = bA[seq * 576 + j + 64 * r];
    fft9<1>(v);
#pragma unroll
    for (int r = 0; r < 9; r++) bB[seq * 576 + j * 9 + r] = v[r];
  }
  __syncthreads();
  for (int task = t; task < 288; task += 256) {
    const int seq = task / 72, j = task % 72, k = j % 9;
    cf v[8];
    v[0] = bB[seq * 576 + j];
#pragma unroll
    for (int r = 1; r < 8; r++) v[r] = cmul(bB[seq * 576 + j + 72 * r], tw<1>(sTW, 8 * k * r));
    fft8<1>(v);
#pragma unroll
    for (int r = 0; r < 8; r++) bA[seq * 576 + (j / 9) * 72 + k + 9 * r] = v[r];
  }
  __syncthreads();
  for (int task = t; task < 288; task += 256) {
    const int seq = task / 72, j = task % 72;
    cf v[8];
    v[0] = bA[seq * 576 + j];
#pragma unroll
    for (int r = 1; r < 8; r++) v[r] = cmul(bA[seq * 576 + j + 72 * r], tw<1>(sTW, j * r));
    fft8<1>(v);
#pragma unroll
    for (int r = 0; r < 8; r++) bB[seq * 576 + j + 72 * r] = v[r];
  }
  __syncthreads();
  TSTAMP(2, 2);
  for (int task = t; task < 4 * DF_; task += 256) {
    const int k = task >> 2, p = task & 3;
    cf z1 = bB[p * 576 + k], z2 = bB[p * 576 + (k == 0 ? 0 : 576 - k)];
    float4 o;
    o.x = 0.5f * (z1.x + z2.x); o.y = 0.5f * (z1.y - z2.y);
    o.z = 0.5f * (z1.y + z2.y); o.w = -0.5f * (z1.x - z2.x);
    *reinterpret_cast<float4*>(X1T + (size_t)k * S_ + row0 + 2 * p) = o;
  }
  TSTAMP(2, 3);
}

__global__ __launch_bounds__(256) void k2_colfft(cf* __restrict__ X, const uint32_t* __restrict__ WT) {
  __shared__ cf sA[2048];
  __shared__ cf sB[2048];
  const int t = threadIdx.x, col = blockIdx.x;
  cf* base = X + (size_t)col * S_;
  const uint32_t* wc = WT + (size_t)col * S_;
  cf v[8];
  TSTAMP(3, 0);
  uint32_t wv8[8];
#pragma unroll
  for (int r = 0; r < 8; r++) v[r] = base[t + 256 * r];
  __shared__ __attribute__((aligned(16))) float sTW[4096];
#pragma unroll
  for (int i = 0; i < 4; i++) reinterpret_cast<float4*>(sTW)[t + 256 * i] = reinterpret_cast<const float4*>(TW2048)[t + 256 * i];
#pragma unroll
  for (int jj = 0; jj < 2; jj++)
#pragma unroll
    for (int r = 0; r < 4; r++) wv8[jj * 4 + r] = wc[t + 256 * jj + 512 * r];
  {
    fft8<1>(v);
#pragma unroll
    for (int r = 0; r < 8; r++) sA[t * 8 + r] = v[r];
  }
  __syncthreads();
  TSTAMP(3, 1);
  {
    const int k = t & 7;
    v[0] = sA[t];
#pragma unroll
    for (int r = 1; r < 8; r++) v[r] = cmul(sA[t + 256 * r], tw<1>(sTW, 32 * k * r));
    fft8<1>(v);
#pragma unroll
    for (int r = 0; r < 8; r++) sB[(t >> 3) * 64 + k + 8 * r] = v[r];
  }
  __syncthreads();
  {
    const int k = t & 63;
    v[0] = sB[t];
#pragma unroll
    for (int r = 1; r < 8; r++) v[r] = cmul(sB[t + 256 * r], tw<1>(sTW, 4 * k * r));
    fft8<1>(v);
#pragma unroll
    for (int r = 0; r < 8; r++) sA[(t >> 6) * 512 + k + 64 * r] = v[r];
  }
  __syncthreads();
  {
#pragma unroll
    for (int jj = 0; jj < 2; jj++) {
      const int j = t + 256 * jj;
      cf* u = v + 4 * jj;
      u[0] = sA[j];
#pragma unroll
      for (int r = 1; r < 4; r++) u[r] = cmul(sA[j + 512 * r], tw<1>(sTW, j * r));
      fft4<1>(u[0], u[1], u[2], u[3]);
#pragma unroll
      for (int r = 0; r < 4; r++) {
        uint32_t wv = wv8[jj * 4 + r];
        cf wcx = cf{bf2f((u16)(wv & 0xffff)), bf2f((u16)(wv >> 16))};
        u[r] = cmul(u[r], wcx);
      }
      fft4<-1>(u[0], u[1], u[2], u[3]);
    }
    __syncthreads();
#pragma unroll
    for (int jj = 0; jj < 2; jj++) {
      const int j = t + 256 * jj;
#pragma unroll
      for (int r = 0; r < 4; r++) sB[j * 4 + r] = v[4 * jj + r];
    }
  }
  __syncthreads();
  {
    const int k = t & 3;
    v[0] = sB[t];
#pragma unroll
    for (int r = 1; r < 8; r++) v[r] = cmul(sB[t + 256 * r], tw<-1>(sTW, 64 * k * r));
    fft8<-1>(v);
#pragma unroll
    for (int r = 0; r < 8; r++) sA[(t >> 2) * 32 + k + 4 * r] = v[r];
  }
  __syncthreads();
  {
    const int k = t & 31;
    v[0] = sA[t];
#pragma unroll
    for (int r = 1; r < 8; r++) v[r] = cmul(sA[t + 256 * r], tw<-1>(sTW, 8 * k * r));
    fft8<-1>(v);
#pragma unroll
    for (int r = 0; r < 8; r++) sB[(t >> 5) * 256 + k + 32 * r] = v[r];
  }
  __syncthreads();
  TSTAMP(3, 2);
  {
    v[0] = sB[t];
#pragma unroll
    for (int r = 1; r < 8; r++) v[r] = cmul(sB[t + 256 * r], tw<-1>(sTW, t * r));
    fft8<-1>(v);
    const float sc = 1.f / 2048.f;
#pragma unroll
    for (int r = 0; r < 8; r++) base[t + 256 * r] = cf{v[r].x * sc, v[r].y * sc};
  }
  TSTAMP(3, 3);
}

__global__ __launch_bounds__(256) void k3_irow_ln(const cf* __restrict__ X2T, const u16* __restrict__ x,
                                                  const float* __restrict__ modp, u16* __restrict__ x1o,
                                                  u16* __restrict__ h2o) {
  __shared__ cf bA[4 * 576];
  __shared__ cf bB[4 * 576];
  const int t = threadIdx.x, w = t >> 6, l = t & 63;
  const int row0 = blockIdx.x * 8;
  __shared__ float smod[1728];
  __shared__ __attribute__((aligned(16))) float sTW[1152];
  u16 xr[2][9];
#pragma unroll
  for (int rr = 0; rr < 2; rr++)
#pragma unroll
    for (int i = 0; i < 9; i++) xr[rr][i] = x[(row0 + 2 * w + rr) * D_ + l + 64 * i];
  {
    float mv[7];
#pragma unroll
    for (int j = 0; j < 7; j++) { const int i = t + 256 * j; mv[j] = modval(modp, 1152 + (i < 1728 ? i : 0)); }
#pragma unroll
    for (int j = 0; j < 7; j++) { const int i = t + 256 * j; if (i < 1728) smod[i] = i < 1152 ? mv[j] : rbf(1.f + mv[j]); }
  }
  for (int i = t; i < 288; i += 256) reinterpret_cast<float4*>(sTW)[i] = reinterpret_cast<const float4*>(TW576)[i];
  for (int task = t; task < 4 * DF_; task += 256) {
    const int k = task >> 2, p = task & 3;
    float4 ab = *reinterpret_cast<const float4*>(X2T + (size_t)k * S_ + row0 + 2 * p);
    if (k == 0 || k == 288) { ab.y = 0.f; ab.w = 0.f; }
    bA[p * 576 + k] = cf{ab.x - ab.w, ab.y + ab.z};
    if (k > 0 && k < 288) bA[p * 576 + 576 - k] = cf{ab.x + ab.w, -ab.y + ab.z};
  }
  __syncthreads();
  {
    const int seq = t >> 6, j = t & 63;
    cf v[9];
#pragma unroll
    for (int r = 0; r < 9; r++) v[r] = bA[seq * 576 + j + 64 * r];
    fft9<-1>(v);
#pragma unroll
    for (int r = 0; r < 9; r++) bB[seq * 576 + j * 9 + r] = v[r];
  }
  __syncthreads();
  for (int task = t; task < 288; task += 256) {
    const int seq = task / 72, j = task % 72, k = j % 9;
    cf v[8];
    v[0] = bB[seq * 576 + j];
#pragma unroll
    for (int r = 1; r < 8; r++) v[r] = cmul(bB[seq * 576 + j + 72 * r], tw<-1>(sTW, 8 * k * r));
    fft8<-1>(v);
#pragma unroll
    for (int r = 0; r < 8; r++) bA[seq * 576 + (j / 9) * 72 + k + 9 * r] = v[r];
  }
  __syncthreads();
  for (int task = t; task < 288; task += 256) {
    const int seq = task / 72, j = task % 72;
    cf v[8];
    v[0] = bA[seq * 576 + j];
#pragma unroll
    for (int r = 1; r < 8; r++) v[r] = cmul(bA[seq * 576 + j + 72 * r], tw<-1>(sTW, j * r));
    fft8<-1>(v);
#pragma unroll
    for (int r = 0; r < 8; r++) bB[seq * 576 + j + 72 * r] = v[r];
  }
  __syncthreads();
  float ga[9], sb[9], t1[9];
#pragma unroll
  for (int i = 0; i < 9; i++) {
    int n = l + 64 * i;
    ga[i] = smod[n];
    sb[i] = smod[576 + n];
    t1[i] = smod[1152 + n];
  }
#pragma unroll
  for (int rr = 0; rr < 2; rr++) {
    const int row = row0 + 2 * w + rr;
    float xv[9];
    float s = 0.f;
#pragma unroll
    for (int i = 0; i < 9; i++) {
      cf z = bB[w * 576 + l + 64 * i];
      float y = rbf((rr == 0 ? z.x : z.y) * (1.f / 576.f));
      float xin = bf2f(xr[rr][i]);
      xv[i] = rbf(xin + rbf(ga[i] * y));
      s += xv[i];
    }
    s = wave_sum(s);
    const float mean = s * (1.f / 576.f);
    float q = 0.f;
#pragma unroll
    for (int i = 0; i < 9; i++) { float d = xv[i] - mean; q += d * d; }
    q = wave_sum(q);
    const float m = rbf(mean), v = rbf(q * (1.f / 576.f));
    const float sq = rbf(sqrtf(rbf(v + 1e-5f)));
#pragma unroll
    for (int i = 0; i < 9; i++) {
      float d = rbf(xv[i] - m);
      float z = rbf(d / sq);
      float h = rbf(rbf(z * t1[i]) + sb[i]);
      x1o[row * D_ + l + 64 * i] = f2bf(xv[i]);
      h2o[row * D_ + l + 64 * i] = f2bf(h);
    }
  }
}

__device__ __forceinline__ int swap23(int k) { return (k & ~12) | ((k & 4) << 1) | ((k & 8) >> 1); }

typedef __attribute__((address_space(3))) bf16x4 lds_bf16x4;

__device__ __forceinline__ void glds16(const void* sbase, uint32_t voff, uint32_t lds_dst) {
  uint32_t keep;
  asm volatile("s_mov_b32 %0, m0\n\ts_mov_b32 m0, %3\n\ts_nop 0\n\tglobal_load_lds_dwordx4 %1, %2\n\ts_mov_b32 m0, %0"
               : "=&s"(keep) : "v"(voff), "s"(sbase), "s"(lds_dst) : "memory");
}
template <int N> __device__ __forceinline__ void waitv() { asm volatile("s_waitcnt vmcnt(%0)" ::"n"(N) : "memory"); }
__device__ __forceinline__ void rawbar() { __builtin_amdgcn_s_barrier(); }

template <int BM> struct StageAddr {
  uint32_t aoff[BM / 64];
  uint32_t boff[3];
};
template <int BM>
__device__ __forceinline__ void stage_addr_init(StageAddr<BM>& s, int lda, int ldb, int wave, int l) {
#pragma unroll
  for (int i = 0; i < BM / 64; i++) {
    const int c = (wave * (BM / 64) + i) * 64 + l, row = c >> 3, slot = c & 7, lc = slot ^ ((row >> 1) & 7);
    s.aoff[i] = (row * lda + lc * 8) * 2;
  }
#pragma unroll
  for (int i = 0; i < 3; i++) {
    const int bi = (i < 2) ? (wave + 8 * i) : (16 + (wave & 1));
    const int c = bi * 64 + l, rho = c / 18, ch = c % 18, k = swap23(rho & 63);
    s.boff[i] = (k * ldb + ch * 8) * 2;
  }
}
template <int BM>
__device__ __forceinline__ void stage_issue(const StageAddr<BM>& s, uint32_t sbase, const u16* Ag, const u16* Bg, int wave) {
#pragma unroll
  for (int i = 0; i < BM / 64; i++) glds16(Ag, s.aoff[i], sbase + (wave * (BM / 64) + i) * 1024);
  const uint32_t sb = sbase + BM * 128;
  glds16(Bg, s.boff[0], sb + wave * 1024);
  glds16(Bg, s.boff[1], sb + (wave + 8) * 1024);
  if (wave < 2) glds16(Bg, s.boff[2], sb + (wave + 16) * 1024);
}

template <int MI>
__device__ __forceinline__ void read_a(const char* sA, int arow0, int ks, int l, bf16x8 (&a)[MI]) {
#pragma unroll
  for (int mi = 0; mi < MI; mi++) {
    const int row = arow0 + mi * 16 + (l & 15);
    const int lc = ks * 4 + (l >> 4), slot = lc ^ ((row >> 1) & 7);
    a[mi] = *reinterpret_cast<const bf16x8*>(sA + row * 128 + slot * 16);
  }
}
__device__ __forceinline__ bf16x8 read_b(const char* p0, const char* p1, int nj) {
  bf16x4 lo = __builtin_amdgcn_ds_read_tr16_b64_v4i16((lds_bf16x4*)(p0 + nj * 32));
  bf16x4 hi = __builtin_amdgcn_ds_read_tr16_b64_v4i16((lds_bf16x4*)(p1 + nj * 32));
  return __builtin_shufflevector(lo, hi, 0, 1, 2, 3, 4, 5, 6, 7);
}

template <int BM, int MI, int NST, int NK>
__device__ __forceinline__ void gemm_mainloop(char* smem, const u16* Ag, int lda, const u16* Bg, int ldb, int wave, int l,
                                              f32x4 (&acc)[MI][9]) {
  constexpr int STAGE = BM * 128 + 64 * 288;
  constexpr int MW = BM / (16 * MI);
  constexpr int NA = BM / 64;
  const int wm = wave % MW, kh = wave / MW;
  const uint32_t lds0 = (uint32_t)(uintptr_t)(__attribute__((address_space(3))) char*)smem;
  StageAddr<BM> sa;
  stage_addr_init<BM>(sa, lda, ldb, wave, l);
#pragma unroll
  for (int p = 0; p < NST; p++) stage_issue<BM>(sa, lds0 + p * STAGE, Ag + p * 64, Bg + (size_t)p * 64 * ldb, wave);
  const int kk = kh * 32 + 8 * (l >> 4) + ((l & 15) >> 2);
  const int bo0 = BM * 128 + swap23(kk) * 288 + (l & 3) * 8;
  const int bo1 = BM * 128 + swap23(kk + 4) * 288 + (l & 3) * 8;
  const int arow0 = wm * 16 * MI;
  if (wave < 2) waitv<(NST - 1) * (NA + 3)>(); else waitv<(NST - 1) * (NA + 2)>();
  rawbar();
  TSTAMP(MI == 4 ? 0 : 1, 1);
  bf16x8 a[MI], b[9];
  read_a<MI>(smem, arow0, kh, l, a);
#pragma unroll
  for (int nj = 0; nj < 9; nj++) b[nj] = read_b(smem + bo0, smem + bo1, nj);
  int buf = 0;
#pragma unroll 1
  for (int kt = 0; kt < NK - 1; kt++) {
    const int ahead = (NK - 2 - kt) < (NST - 2) ? (NK - 2 - kt) : (NST - 2);
    if (NST >= 4 && ahead >= 2) { if (wave < 2) waitv<2 * (NA + 3)>(); else waitv<2 * (NA + 2)>(); }
    else if (ahead == 1) { if (wave < 2) waitv<NA + 3>(); else waitv<NA + 2>(); }
    else waitv<0>();
    asm volatile("s_waitcnt lgkmcnt(0)" ::: "memory");
    rawbar();
    if (kt + NST < NK) stage_issue<BM>(sa, lds0 + buf * STAGE, Ag + (kt + NST) * 64, Bg + (size_t)(kt + NST) * 64 * ldb, wave);
    buf++; if (buf == NST) buf = 0;
    const char* nxt = smem + buf * STAGE;
    bf16x8 an[MI];
    read_a<MI>(nxt, arow0, kh, l, an);
#pragma unroll
    for (int nj = 0; nj < 9; nj++) {
#pragma unroll
      for (int mi = 0; mi < MI; mi++) acc[mi][nj] = __builtin_amdgcn_mfma_f32_16x16x32_bf16(b[nj], a[mi], acc[mi][nj], 0, 0, 0);
      b[nj] = read_b(nxt + bo0, nxt + bo1, nj);
    }
#pragma unroll
    for (int mi = 0; mi < MI; mi++) a[mi] = an[mi];
  }
#pragma unroll
  for (int nj = 0; nj < 9; nj++)
#pragma unroll
    for (int mi = 0; mi < MI; mi++) acc[mi][nj] = __builtin_amdgcn_mfma_f32_16x16x32_bf16(b[nj], a[mi], acc[mi][nj], 0, 0, 0);
  asm volatile("s_waitcnt lgkmcnt(0)" ::: "memory");
  rawbar();
  TSTAMP(MI == 4 ? 0 : 1, 2);
}

__device__ __forceinline__ uint32_t cvt_pk_bf16(float a, float b) {
  uint32_t r;
  asm("v_cvt_pk_bf16_f32 %0, %1, %2" : "=v"(r) : "v"(a), "v"(b));
  return r;
}
__device__ __forceinline__ float silu_fast(float h) {
  return h * __builtin_amdgcn_rcpf(1.f + __builtin_amdgcn_exp2f(h * -1.44269504088896340736f));
}

__global__ __launch_bounds__(512) void k4_gemm1(const u16* __restrict__ A, const u16* __restrict__ B, u16* __restrict__ H) {
  constexpr int BM = 256, MI = 4, NST = 3;
  __shared__ __attribute__((aligned(16))) char smem[NST * (BM * 128 + 64 * 288)];
  const int tid = threadIdx.x, wave = __builtin_amdgcn_readfirstlane(tid >> 6), l = tid & 63;
  const int xcd = blockIdx.x & 7, idx = blockIdx.x >> 3;
  const int nt = 4 * xcd + (idx & 3), mt = idx >> 2;
  TSTAMP(0, 0);
  f32x4 acc[MI][9];
#pragma unroll
  for (int i = 0; i < MI; i++)
#pragma unroll
    for (int j = 0; j < 9; j++) acc[i][j] = f32x4{0.f, 0.f, 0.f, 0.f};
  gemm_mainloop<BM, MI, NST, 9>(smem, A + (size_t)mt * BM * D_, D_, B + nt * 144, F_, wave, l, acc);
  const int wm = wave & 3, kh = wave >> 2;
  f32x4* xb = reinterpret_cast<f32x4*>(smem);
#pragma unroll
  for (int i = 0; i < 2; i++)
#pragma unroll
    for (int nj = 0; nj < 9; nj++) xb[((wave * 2 + i) * 9 + nj) * 64 + l] = kh == 0 ? acc[2 + i][nj] : acc[i][nj];
  __syncthreads();
  f32x4 o[2][9];
#pragma unroll
  for (int i = 0; i < 2; i++)
#pragma unroll
    for (int nj = 0; nj < 9; nj++) o[i][nj] = xb[(((wave ^ 4) * 2 + i) * 9 + nj) * 64 + l] + (kh == 0 ? acc[i][nj] : acc[2 + i][nj]);
  __syncthreads();
  constexpr int PIT = 304;
  char* stg = smem + wave * 32 * PIT;
#pragma unroll
  for (int i = 0; i < 2; i++)
#pragma unroll
    for (int nj = 0; nj < 9; nj++) {
      uint32_t p0 = cvt_pk_bf16(o[i][nj][0], o[i][nj][1]), p1 = cvt_pk_bf16(o[i][nj][2], o[i][nj][3]);
      uint2 pk;
      pk.x = cvt_pk_bf16(silu_fast(__uint_as_float(p0 << 16)), silu_fast(__uint_as_float(p0 & 0xffff0000u)));
      pk.y = cvt_pk_bf16(silu_fast(__uint_as_float(p1 << 16)), silu_fast(__uint_as_float(p1 & 0xffff0000u)));
      *reinterpret_cast<uint2*>(stg + (i * 16 + (l & 15)) * PIT + nj * 32 + (l >> 4) * 8) = pk;
    }
  u16* Hg = H + ((size_t)mt * BM + wm * 64 + kh * 32) * F_ + nt * 144;
#pragma unroll
  for (int it = 0; it < 9; it++) {
    const int c = it * 64 + l, row = c / 18, ch = c % 18;
    uint4 v = *reinterpret_cast<const uint4*>(stg + row * PIT + ch * 16);
    *reinterpret_cast<uint4*>(Hg + (size_t)row * F_ + ch * 8) = v;
  }
  TSTAMP(0, 3);
}

__global__ __launch_bounds__(512) void k5_gemm2(const u16* __restrict__ A, const u16* __restrict__ B, float* __restrict__ P) {
  constexpr int BM = 128, MI = 2, NST = 4;
  __shared__ __attribute__((aligned(16))) char smem[NST * (BM * 128 + 64 * 288)];
  const int tid = threadIdx.x, wave = __builtin_amdgcn_readfirstlane(tid >> 6), l = tid & 63;
  const int xcd = blockIdx.x & 7, idx = blockIdx.x >> 3;
  const int ks = xcd >> 1, mt = (xcd & 1) * 8 + (idx & 7), nt = idx >> 3;
  TSTAMP(1, 0);
  f32x4 acc[MI][9];
#pragma unroll
  for (int i = 0; i < MI; i++)
#pragma unroll
    for (int j = 0; j < 9; j++) acc[i][j] = f32x4{0.f, 0.f, 0.f, 0.f};
  gemm_mainloop<BM, MI, NST, 18>(smem, A + (size_t)mt * BM * F_ + ks * 1152, F_, B + (size_t)ks * 1152 * D_ + nt * 144, D_, wave, l, acc);
  const int wm = wave & 3, kh = wave >> 2;
  f32x4* xb = reinterpret_cast<f32x4*>(smem);
#pragma unroll
  for (int nj = 0; nj < 9; nj++) xb[(wave * 9 + nj) * 64 + l] = acc[kh == 0 ? 1 : 0][nj];
  __syncthreads();
  float* Pg = P + (size_t)ks * S_ * D_ + (size_t)mt * BM * D_ + nt * 144;
#pragma unroll
  for (int nj = 0; nj < 9; nj++) {
    const int mi = kh == 0 ? 0 : 1;
    f32x4 o = xb[((wave ^ 4) * 9 + nj) * 64 + l];
    o += acc[mi][nj];
    const int m = wm * 32 + mi * 16 + (l & 15), n = nj * 16 + (l >> 4) * 4;
    *reinterpret_cast<f32x4*>(Pg + (size_t)m * D_ + n) = o;
  }
  TSTAMP(1, 3);
}

__global__ __launch_bounds__(256) void k6_final(const float* __restrict__ P, const u16* __restrict__ x1,
                                                const float* __restrict__ modp, u16* __restrict__ out) {
  const int i = (blockIdx.x * 256 + threadIdx.x) * 4;
  if (i >= S_ * D_) return;
  const int n = i % D_;
  f32x4 s = *reinterpret_cast<const f32x4*>(P + i);
  s += *reinterpret_cast<const f32x4*>(P + (size_t)S_ * D_ + i);
  s += *reinterpret_cast<const f32x4*>(P + (size_t)2 * S_ * D_ + i);
  s += *reinterpret_cast<const f32x4*>(P + (size_t)3 * S_ * D_ + i);
  uint2 xv = *reinterpret_cast<const uint2*>(x1 + i);
  u16 xs[4] = {(u16)(xv.x & 0xffff), (u16)(xv.x >> 16), (u16)(xv.y & 0xffff), (u16)(xv.y >> 16)};
  u16 o[4];
#pragma unroll
  for (int r = 0; r < 4; r++) {
    float gb = modval(modp, 5 * 576 + n + r);
    o[r] = f2bf(bf2f(xs[r]) + rbf(gb * rbf(s[r])));
  }
  uint2 pk;
  pk.x = (uint32_t)o[0] | ((uint32_t)o[1] << 16);
  pk.y = (uint32_t)o[2] | ((uint32_t)o[3] << 16);
  *reinterpret_cast<uint2*>(out + i) = pk;
}

extern "C" void kernel_launch(void* const* d_in, const int* in_sizes, int n_in, void* d_out, int out_size, void* d_ws,
                              size_t ws_size, hipStream_t stream) {
  const u16* A_x = (const u16*)d_in[0];
  const u16* w_r = (const u16*)d_in[1];
  const u16* w_i = (const u16*)d_in[2];
  const u16* w1 = (const u16*)d_in[4];
  const u16* w2 = (const u16*)d_in[5];
  const u16* c = (const u16*)d_in[6];
  const u16* wmod = (const u16*)d_in[7];
  u16* out = (u16*)d_out;
  char* ws = (char*)d_ws;
  size_t off = 0;
  auto carve = [&](size_t bytes) { char* p = ws + off; off += (bytes + 255) & ~(size_t)255; return p; };
  float* modp = (float*)carve(4 * 3456 * 4);
  uint32_t* WT = (uint32_t*)carve((size_t)DF_ * S_ * 4);
  cf* X1T = (cf*)carve((size_t)DF_ * S_ * 8);
  u16* x1 = (u16*)carve((size_t)S_ * D_ * 2);
  u16* h2 = (u16*)carve((size_t)S_ * D_ * 2);
  u16* H = (u16*)carve((size_t)S_ * F_ * 2);
  float* P = (float*)carve((size_t)4 * S_ * D_ * 4);
  k0_prep<<<108 + 64, 256, 0, stream>>>(c, wmod, w_r, w_i, modp, WT);
  k1_ln_rowfft<<<256, 256, 0, stream>>>(A_x, modp, X1T);
  k2_colfft<<<DF_, 256, 0, stream>>>(X1T, WT);
  k3_irow_ln<<<256, 256, 0, stream>>>(X1T, A_x, modp, x1, h2);
  k4_gemm1<<<256, 512, 0, stream>>>(h2, w1, H);
  k5_gemm2<<<256, 512, 0, stream>>>(H, w2, P);
  k6_final<<<(S_ * D_ / 4 + 255) / 256, 256, 0, stream>>>(P, x1, modp, out);
}
```
